```python
import torch

def _softmax(x, axis=-1):
    return torch.softmax(x, dim=axis)

def _gelu(x):
    return torch.nn.functional.gelu(x, approximate='tanh')

def _silu(x):
    return torch.nn.functional.silu(x)

def _sigmoid(x):
    return torch.sigmoid(x)

def _layernorm(x, axis=-1, eps=1e-05):
    m = x.mean(dim=axis, keepdim=True)
    v = x.var(dim=axis, keepdim=True, unbiased=False)
    return (x - m) / torch.sqrt(v + eps)

def _scatter_add(tgt, idx, src, axis=0):
    return tgt.clone().index_add_(axis, idx, src)

def _segment_sum(src, seg, n):
    out = torch.zeros((n,) + src.shape[1:], dtype=src.dtype)
    return out.index_add_(0, seg, src)

def _pad(x, pw):
    flat = tuple((v for pair in reversed(pw) for v in pair))
    return torch.nn.functional.pad(x, flat)
kernel_name = 'adaLN_cond__memory_aug_recurrent_mem_tr__dense_activation_squared_relu__prompt'

def setup_inputs():

    _w = lambda a: (lambda t: t.to(torch.bfloat16) if t.is_floating_point() else t)(torch.as_tensor(a))

    def _A_setup():
        B, S, Dm = (1, 2048, 640)
        H, Dh = (32, 192)
        M, seg = (8, 32)
        seg = min(seg, S)
        S = S // seg * seg
        return {'x': _w((lambda *s: torch.randn(*s, dtype=torch.bfloat16) * 0.05)(B, S, Dm)), 'mem0': _w((lambda *s: torch.randn(*s, dtype=torch.bfloat16) * 0.4)(B, M, Dm)), 'Wq': _w((lambda *s: torch.randn(*s, dtype=torch.bfloat16) * 3.2)(Dm, H, Dh) * Dm ** (-0.5)), 'Wk': _w((lambda *s: torch.randn(*s, dtype=torch.bfloat16) * 3.2)(Dm, H, Dh) * Dm ** (-0.5)), 'Wv': _w((lambda *s: torch.randn(*s, dtype=torch.bfloat16) * 0.4)(Dm, H, Dh) * Dm ** (-0.5)), 'Wo': _w((lambda *s: torch.randn(*s, dtype=torch.bfloat16) * 0.4)(H, Dh, Dm) * (H * Dh) ** (-0.5)), 'seg_len': seg}
    _A_in = _A_setup()

    def _B_setup():
        B, S, D, F = (1, 2048, 640, 1536)
        return {'x': _w((lambda *s: torch.randn(*s, dtype=torch.bfloat16) * 0.05)(B, S, D)), 'w1': _w((lambda *s: torch.randn(*s, dtype=torch.bfloat16) * 0.05)(D, F)), 'b1': _w((lambda *s: torch.randn(*s, dtype=torch.bfloat16) * 0.4)(F)), 'w2': _w((lambda *s: torch.randn(*s, dtype=torch.bfloat16) * 0.05)(F, D)), 'b2': _w((lambda *s: torch.randn(*s, dtype=torch.bfloat16) * 3.2)(D))}
    _B_in = _B_setup()
    return {'A_x': _A_in['x'], 'A_mem0': _A_in['mem0'], 'A_Wq': _A_in['Wq'], 'A_Wk': _A_in['Wk'], 'A_Wv': _A_in['Wv'], 'A_Wo': _A_in['Wo'], 'A_seg_len': _A_in['seg_len'], 'B_x': _B_in['x'], 'B_w1': _B_in['w1'], 'B_b1': _B_in['b1'], 'B_w2': _B_in['w2'], 'B_b2': _B_in['b2'], 'c': _w((lambda *s: torch.randn(*s, dtype=torch.bfloat16) * 0.1)(1, 640) * 8.0), 'w_mod': _w((lambda *s: torch.randn(*s, dtype=torch.bfloat16) * 0.1)(640, 6 * 640) * 640 ** (-0.5) * 8.0)}

def reference(A_x, A_mem0, A_Wq, A_Wk, A_Wv, A_Wo, A_seg_len, B_x, B_w1, B_b1, B_w2, B_b2, c, w_mod):

    def _A_ref(x, mem0, Wq, Wk, Wv, Wo, seg_len):
        B, S, Dm = x.shape
        H, Dh = (Wq.shape[1], Wq.shape[2])
        M = mem0.shape[1]
        scale = Dh ** (-0.5)
        n_seg = S // seg_len
        L = M + seg_len
        idx = torch.arange(L)
        mask = (idx[:, None] < M) | (idx[:, None] >= idx[None, :])
        mem = mem0
        outs = []
        for s in range(n_seg):
            xs = x[:, s * seg_len:(s + 1) * seg_len, :].clone()
            h = torch.concatenate([mem, xs], axis=1)
            q = torch.einsum('btd,dhe->bhte', h, Wq)
            k = torch.einsum('btd,dhe->bhte', h, Wk)
            v = torch.einsum('btd,dhe->bhte', h, Wv)
            scores = torch.einsum('bhqe,bhke->bhqk', q, k) * scale
            scores = torch.where(mask[None, None], scores, -1000000000.0)
            p = _softmax(scores, axis=-1)
            o = torch.einsum('bhqk,bhke->bhqe', p, v)
            o = torch.einsum('bhte,hed->btd', o, Wo)
            mem = o[:, :M, :].clone()
            outs.append(o[:, M:, :])
        return torch.concatenate(outs, axis=1)

    def _B_ref(x, w1, b1, w2, b2):
        h = torch.einsum('bsd,df->bsf', x, w1) + b1
        zero = torch.zeros_like(h)
        r = torch.where(h > zero, h, zero)
        h = r * r
        out = torch.einsum('bsf,fd->bsd', h, w2) + b2
        return out
    x = A_x
    mod = (_silu(c) @ w_mod).reshape(c.shape[0], 6, 1, -1)
    sa, ca, ga, sb, cb, gb = (mod[:, 0].clone(), mod[:, 1].clone(), mod[:, 2].clone(), mod[:, 3].clone(), mod[:, 4].clone(), mod[:, 5].clone())
    h = _layernorm(x, axis=-1) * (1 + ca) + sa
    x = x + ga * _A_ref(h, A_mem0, A_Wq, A_Wk, A_Wv, A_Wo, A_seg_len)
    h = _layernorm(x, axis=-1) * (1 + cb) + sb
    x = x + gb * _B_ref(h, B_w1, B_b1, B_w2, B_b2)
    return x
```

```cpp
#include <hip/hip_runtime.h>
#include <hip/hip_bf16.h>
typedef __hip_bfloat16 bf16;
__device__ __forceinline__ float b2f(bf16 v) { return __bfloat162float(v); }
__device__ __forceinline__ bf16 f2b(float v) { return __float2bfloat16(v); }
__device__ __forceinline__ float rb(float v) { return b2f(f2b(v)); }

constexpr int S = 2048, D = 640, H = 32, DH = 192, M = 8, SEG = 32, NSEG = 64, L = 40, F = 1536, HD = H * DH;

__global__ void k_mod(const bf16* c, const bf16* wmod, float* mod) {
    int j = blockIdx.x * blockDim.x + threadIdx.x; if (j >= 6 * D) return;
    float acc = 0;
    for (int k = 0; k < D; ++k) { float x = b2f(c[k]); float s = rb(x / (1.f + __expf(-x))); acc += s * b2f(wmod[(size_t)k * 6 * D + j]); }
    mod[j] = rb(acc);
}
__global__ void k_ln(const bf16* x, const float* sh, const float* sc, bf16* h) {
    int row = blockIdx.x, t = threadIdx.x; __shared__ float red[64];
    float v[10]; float s = 0;
    for (int i = 0; i < 10; ++i) { v[i] = b2f(x[(size_t)row * D + t + 64 * i]); s += v[i]; }
    for (int o = 32; o; o >>= 1) s += __shfl_xor(s, o);
    float mean = s / D; float q = 0;
    for (int i = 0; i < 10; ++i) { float d = v[i] - mean; q += d * d; }
    for (int o = 32; o; o >>= 1) q += __shfl_xor(q, o);
    float m = rb(mean), var = rb(q / D); float sd = rb(sqrtf(rb(var + 1e-5f)));
    for (int i = 0; i < 10; ++i) { int cidx = t + 64 * i; float n = rb(rb(v[i] - m) / sd); h[(size_t)row * D + cidx] = f2b(rb(n * rb(1.f + sc[cidx])) + sh[cidx]); }
}
template <int MODE>
__global__ void k_gemm(const bf16* A, int lda, const bf16* B, int ldb, bf16* C, int ldc, int Mr, int N, int K, const bf16* bias, const float* gate, const bf16* resid) {
    __shared__ float As[16][65], Bs[16][65];
    int tx = threadIdx.x & 15, ty = threadIdx.x >> 4; int m0 = blockIdx.y * 64, n0 = blockIdx.x * 64;
    float acc[4][4] = {};
    for (int k0 = 0; k0 < K; k0 += 16) {
        for (int i = threadIdx.x; i < 1024; i += 256) { int r = i >> 4, kk = i & 15; As[kk][r] = (m0 + r < Mr) ? b2f(A[(size_t)(m0 + r) * lda + k0 + kk]) : 0.f; }
        for (int i = threadIdx.x; i < 1024; i += 256) { int kk = i >> 6, cc = i & 63; Bs[kk][cc] = b2f(B[(size_t)(k0 + kk) * ldb + n0 + cc]); }
        __syncthreads();
        for (int kk = 0; kk < 16; ++kk) { float a[4], b[4];
            for (int i = 0; i < 4; ++i) { a[i] = As[kk][ty * 4 + i]; b[i] = Bs[kk][tx * 4 + i]; }
            for (int i = 0; i < 4; ++i) for (int j = 0; j < 4; ++j) acc[i][j] += a[i] * b[j]; }
        __syncthreads();
    }
    for (int i = 0; i < 4; ++i) for (int j = 0; j < 4; ++j) { int r = m0 + ty * 4 + i, cidx = n0 + tx * 4 + j; if (r >= Mr) continue;
        float v = rb(acc[i][j]);
        if (MODE == 1) { v = rb(v + b2f(bias[cidx])); v = v > 0 ? v : 0; v = v * v; }
        if (MODE == 2) { v = rb(v + b2f(bias[cidx])); v = b2f(resid[(size_t)r * ldc + cidx]) + rb(gate[cidx] * v); }
        if (MODE == 3) { v = b2f(resid[(size_t)r * ldc + cidx]) + rb(gate[cidx] * v); }
        C[(size_t)r * ldc + cidx] = f2b(v); }
}
__global__ void k_attn(const bf16* qm, const bf16* km, const bf16* vm, const bf16* qx, const bf16* kx, const bf16* vx, bf16* om, bf16* ox, int seg0, int memrows) {
    int h = blockIdx.x, s = seg0 + blockIdx.y, t = threadIdx.x;
    __shared__ float p[40][41];
    auto Q = [&](int r, int e) { return r < M ? b2f(qm[((size_t)s * M + r) * HD + h * DH + e]) : b2f(qx[((size_t)s * SEG + r - M) * HD + h * DH + e]); };
    auto K = [&](int r, int e) { return r < M ? b2f(km[((size_t)s * M + r) * HD + h * DH + e]) : b2f(kx[((size_t)s * SEG + r - M) * HD + h * DH + e]); };
    auto V = [&](int r, int e) { return r < M ? b2f(vm[((size_t)s * M + r) * HD + h * DH + e]) : b2f(vx[((size_t)s * SEG + r - M) * HD + h * DH + e]); };
    int r0 = memrows ? 0 : M, r1 = memrows ? M : L;
    const float scale = rb(0.07216878364870322f);
    for (int i = t; i < (r1 - r0) * L; i += blockDim.x) { int r = r0 + i / L, j = i % L; float a = 0; for (int e = 0; e < DH; ++e) a += Q(r, e) * K(j, e);
        a = rb(rb(a) * 0.07216878364870322f); if (!(r < M || r >= j)) a = -1e9f; p[r][j] = a; }
    (void)scale;
    __syncthreads();
    for (int r = r0 + t; r < r1; r += blockDim.x) { float mx = -3e38f; for (int j = 0; j < L; ++j) mx = fmaxf(mx, p[r][j]); float sum = 0; for (int j = 0; j < L; ++j) { float e = __expf(p[r][j] - mx); p[r][j] = e; sum += e; } for (int j = 0; j < L; ++j) p[r][j] = rb(p[r][j] / sum); }
    __syncthreads();
    for (int i = t; i < (r1 - r0) * DH; i += blockDim.x) { int r = r0 + i / DH, e = i % DH; float a = 0; for (int j = 0; j < L; ++j) a += p[r][j] * V(j, e);
        if (r < M) om[(size_t)r * HD + h * DH + e] = f2b(a); else ox[((size_t)s * SEG + r - M) * HD + h * DH + e] = f2b(a); }
}
extern "C" void kernel_launch(void* const* d_in, const int* in_sizes, int n_in, void* d_out, int out_size, void* d_ws, size_t ws_size, hipStream_t stream) {
    const bf16* x = (const bf16*)d_in[0]; const bf16* mem0 = (const bf16*)d_in[1];
    const bf16* Wq = (const bf16*)d_in[2]; const bf16* Wk = (const bf16*)d_in[3]; const bf16* Wv = (const bf16*)d_in[4]; const bf16* Wo = (const bf16*)d_in[5];
    const bf16* w1 = (const bf16*)d_in[8]; const bf16* b1 = (const bf16*)d_in[9]; const bf16* w2 = (const bf16*)d_in[10]; const bf16* b2 = (const bf16*)d_in[11];
    const bf16* c = (const bf16*)d_in[12]; const bf16* wmod = (const bf16*)d_in[13];
    char* ws = (char*)d_ws; size_t o = 0; auto take = [&](size_t b) { void* p = ws + o; o += (b + 255) & ~(size_t)255; return p; };
    float* mod = (float*)take(6 * D * 4); bf16* h = (bf16*)take((size_t)S * D * 2);
    bf16* qx = (bf16*)take((size_t)S * HD * 2); bf16* kx = (bf16*)take((size_t)S * HD * 2); bf16* vx = (bf16*)take((size_t)S * HD * 2); bf16* ox = (bf16*)take((size_t)S * HD * 2);
    bf16* qm = (bf16*)take((size_t)NSEG * M * HD * 2); bf16* km = (bf16*)take((size_t)NSEG * M * HD * 2); bf16* vm = (bf16*)take((size_t)NSEG * M * HD * 2);
    bf16* om = (bf16*)take((size_t)M * HD * 2); bf16* mem = (bf16*)take((size_t)M * D * 2); bf16* x1 = (bf16*)take((size_t)S * D * 2); bf16* h2 = (bf16*)take((size_t)S * D * 2); bf16* hid = (bf16*)take((size_t)S * F * 2);
    k_mod<<<15, 256, 0, stream>>>(c, wmod, mod);
    k_ln<<<S, 64, 0, stream>>>(x, mod, mod + D, h);
    k_gemm<0><<<dim3(HD / 64, S / 64), 256, 0, stream>>>(h, D, Wq, HD, qx, HD, S, HD, D, nullptr, nullptr, nullptr);
    k_gemm<0><<<dim3(HD / 64, S / 64), 256, 0, stream>>>(h, D, Wk, HD, kx, HD, S, HD, D, nullptr, nullptr, nullptr);
    k_gemm<0><<<dim3(HD / 64, S / 64), 256, 0, stream>>>(h, D, Wv, HD, vx, HD, S, HD, D, nullptr, nullptr, nullptr);
    for (int s = 0; s < NSEG; ++s) {
        const bf16* m = s ? mem : mem0;
        k_gemm<0><<<dim3(HD / 64, 1), 256, 0, stream>>>(m, D, Wq, HD, qm + (size_t)s * M * HD, HD, M, HD, D, nullptr, nullptr, nullptr);
        k_gemm<0><<<dim3(HD / 64, 1), 256, 0, stream>>>(m, D, Wk, HD, km + (size_t)s * M * HD, HD, M, HD, D, nullptr, nullptr, nullptr);
        k_gemm<0><<<dim3(HD / 64, 1), 256, 0, stream>>>(m, D, Wv, HD, vm + (size_t)s * M * HD, HD, M, HD, D, nullptr, nullptr, nullptr);
        if (s + 1 < NSEG) {
            k_attn<<<dim3(H, 1), 256, 0, stream>>>(qm, km, vm, qx, kx, vx, om, ox, s, 1);
            k_gemm<0><<<dim3(D / 64, 1), 256, 0, stream>>>(om, HD, Wo, D, mem, D, M, D, HD, nullptr, nullptr, nullptr);
        }
    }
    k_attn<<<dim3(H, NSEG), 256, 0, stream>>>(qm, km, vm, qx, kx, vx, om, ox, 0, 0);
    k_gemm<3><<<dim3(D / 64, S / 64), 256, 0, stream>>>(ox, HD, Wo, D, x1, D, S, D, HD, nullptr, mod + 2 * D, x);
    k_ln<<<S, 64, 0, stream>>>(x1, mod + 3 * D, mod + 4 * D, h2);
    k_gemm<1><<<dim3(F / 64, S / 64), 256, 0, stream>>>(h2, D, w1, F, hid, F, S, F, D, b1, nullptr, nullptr);
    k_gemm<2><<<dim3(D / 64, S / 64), 256, 0, stream>>>(hid, F, w2, D, (bf16*)d_out, D, S, D, F, b2, mod + 5 * D, x1);
}
```

```cpp
#include <hip/hip_runtime.h>
#include <hip/hip_bf16.h>
#include <stdint.h>

#define LAS __attribute__((address_space(3)))
#define GAS __attribute__((address_space(1)))
#define DEV __device__ __forceinline__
typedef unsigned v4u __attribute__((ext_vector_type(4)));
typedef unsigned v2u __attribute__((ext_vector_type(2)));
typedef unsigned v8u __attribute__((ext_vector_type(8)));
typedef float v4f __attribute__((ext_vector_type(4)));
typedef short v4s __attribute__((ext_vector_type(4)));
typedef __bf16 bf8 __attribute__((ext_vector_type(8)));
typedef __bf16 bf16v __attribute__((ext_vector_type(16)));
typedef __bf16 bf2 __attribute__((ext_vector_type(2)));
typedef __amdgpu_buffer_rsrc_t rsrc_t;

constexpr int D = 640, NH = 32, DH = 192, HD = NH * DH, NSEG = 64, FF = 1536;

constexpr unsigned PAGE = 2u << 20;
constexpr unsigned C_TICKA = 0, C_TICKB = 256, C_ABORT = 512, C_PAR = 768, C_MODA = 1024, C_MODB = 1280;
constexpr unsigned C_FH = 2048;
constexpr unsigned C_FK = 4096, C_FX = 4608, C_FU = 5120, C_FKM = 12288, C_FP = 6144, C_FM1 = 6656, C_FO = 7168, C_FW = 7680, C_FL = 8448;
constexpr unsigned L_MEM = 8192, MEM_SLOT = 16384;
constexpr unsigned L_P = L_MEM + 4 * MEM_SLOT, P_SLOT = 24576;
constexpr unsigned L_WM = L_P + 4 * P_SLOT, WM_SLOT = 327680;
constexpr unsigned L_PROBE0 = L_WM + 2 * WM_SLOT, L_PROBE1 = 2 * WM_SLOT;
constexpr unsigned ZERO_BYTES = PAGE + 2 * (L_PROBE1 + 8192);
constexpr unsigned B_MOD = 2 * PAGE;
constexpr unsigned B_HF = B_MOD + 16384;
constexpr unsigned B_KX = B_HF + NSEG * 40960;
constexpr unsigned B_VX = B_KX + NSEG * NH * 12288;
constexpr unsigned B_KM = B_VX + NSEG * NH * 12288;
constexpr unsigned B_ZX = B_KM + NSEG * NH * 3072;
constexpr unsigned B_ZM = B_ZX + NSEG * 40 * NH * 1024;
constexpr unsigned B_PX = B_ZM + NSEG * 40 * 8192;
constexpr unsigned B_PM = B_PX + NSEG * NH * 2048;
constexpr unsigned B_X1 = B_PM + NSEG * 16384;
constexpr unsigned B_HID = B_X1 + 2048 * D * 2;
constexpr unsigned B_H2 = B_HID + NSEG * 8 * 12288;
constexpr unsigned B_DBG = B_H2 + NSEG * 40960;
constexpr unsigned B_TS = B_DBG + 65 * 10240;
constexpr unsigned B_END = B_TS + 320 * 66 * 8 * 8;

constexpr unsigned OOB = 0x80000000u;
constexpr int LDS_BYTES = 160 * 1024;
constexpr unsigned SPIN_CAP = 1u << 18;
#define POLLSLEEP 8

struct Params {
    const uint16_t* x; const uint16_t* mem0; const uint16_t* Wq; const uint16_t* Wk; const uint16_t* Wv; const uint16_t* Wo;
    const uint16_t* w1; const uint16_t* b1; const uint16_t* w2; const uint16_t* b2; const uint16_t* c; const uint16_t* wmod;
    uint16_t* out; unsigned char* ws;
};

template <int V> struct IC { static constexpr int value = V; };
template <int I, int N, class F> DEV void sfor(F&& f) { if constexpr (I < N) { f(IC<I>{}); sfor<I + 1, N>(f); } }

DEV v4u ld16(rsrc_t r, unsigned off) { return __builtin_amdgcn_raw_buffer_load_b128(r, off, 0, 16); }
DEV v2u ld8(rsrc_t r, unsigned off) { return __builtin_amdgcn_raw_buffer_load_b64(r, off, 0, 16); }
DEV unsigned ld4(rsrc_t r, unsigned off) { return __builtin_amdgcn_raw_buffer_load_b32(r, off, 0, 16); }
DEV void st16(rsrc_t r, unsigned off, v4u v) { __builtin_amdgcn_raw_buffer_store_b128(v, r, off, 0, 16); }
DEV void st4(rsrc_t r, unsigned off, unsigned v) { __builtin_amdgcn_raw_buffer_store_b32(v, r, off, 0, 16); }
DEV v4u ld16s(rsrc_t r, unsigned v, unsigned so) { return __builtin_amdgcn_raw_buffer_load_b128(r, v, so, 16); }
DEV v2u ld8s(rsrc_t r, unsigned v, unsigned so) { return __builtin_amdgcn_raw_buffer_load_b64(r, v, so, 16); }
DEV unsigned ld4s(rsrc_t r, unsigned v, unsigned so) { return __builtin_amdgcn_raw_buffer_load_b32(r, v, so, 16); }
DEV void st16s(rsrc_t r, unsigned v, unsigned so, v4u x) { __builtin_amdgcn_raw_buffer_store_b128(x, r, v, so, 16); }
DEV void st8s(rsrc_t r, unsigned v, unsigned so, v2u x) { __builtin_amdgcn_raw_buffer_store_b64(x, r, v, so, 16); }
DEV void st4s(rsrc_t r, unsigned v, unsigned so, unsigned x) { __builtin_amdgcn_raw_buffer_store_b32(x, r, v, so, 16); }
DEV void drain() { asm volatile("s_waitcnt vmcnt(0)" ::: "memory"); }
DEV void bar() { asm volatile("s_waitcnt lgkmcnt(0)" ::: "memory"); __builtin_amdgcn_s_barrier(); asm volatile("" ::: "memory"); }
DEV float bflo(unsigned u) { return __uint_as_float(u << 16); }
DEV float bfhi(unsigned u) { return __uint_as_float(u & 0xffff0000u); }
DEV unsigned pk2(float a, float b) { bf2 v; v[0] = (__bf16)a; v[1] = (__bf16)b; return __builtin_bit_cast(unsigned, v); }
DEV float rb(float a) { return (float)(__bf16)a; }
DEV unsigned short bf1(float a) { return __builtin_bit_cast(unsigned short, (__bf16)a); }
DEV unsigned nz(unsigned u) { return max(u, 1u); }
DEV v4u nz4(v4u v) { v.x = nz(v.x); v.y = nz(v.y); v.z = nz(v.z); v.w = nz(v.w); return v; }
DEV unsigned min4(v4u v) { return min(min(v.x, v.y), min(v.z, v.w)); }
DEV unsigned rmap(unsigned L, unsigned par) { return ((L >> 13) << 14) + (L & 8191u) + par * 8192u; }
DEV v4f mfma32(v4u a, v4u b, v4f c) { return __builtin_amdgcn_mfma_f32_16x16x32_bf16(__builtin_bit_cast(bf8, a), __builtin_bit_cast(bf8, b), c, 0, 0, 0); }
DEV v4f mfma16(v2u a, v2u b, v4f c) { return __builtin_amdgcn_mfma_f32_16x16x16bf16_1k(__builtin_bit_cast(v4s, a), __builtin_bit_cast(v4s, b), c, 0, 0, 0); }
DEV v4f smf(v4u a, v8u b, v4f c, int idx) { return __builtin_amdgcn_smfmac_f32_16x16x64_bf16(__builtin_bit_cast(bf8, a), __builtin_bit_cast(bf16v, b), c, idx, 0, 0); }
#define SB() __builtin_amdgcn_sched_barrier(0)
DEV void smf_a(v4f& c, v4u a, v8u w, int idx) { asm volatile("v_smfmac_f32_16x16x64_bf16 %0, %1, %2, %3" : "+v"(c) : "v"(a), "a"(w), "v"(idx)); }
DEV void smf_v(v4f& c, v4u a, v8u w, int idx) { asm volatile("v_smfmac_f32_16x16x64_bf16 %0, %1, %2, %3" : "+v"(c) : "v"(a), "v"(w), "v"(idx)); }
DEV void smf_a0(v4f& c, v4u a, v8u w, int idx) { c = (v4f){0, 0, 0, 0}; asm volatile("s_nop 1\n\tv_smfmac_f32_16x16x64_bf16 %0, %1, %2, %3" : "+v"(c) : "v"(a), "a"(w), "v"(idx)); }
DEV void smf_v0(v4f& c, v4u a, v8u w, int idx) { c = (v4f){0, 0, 0, 0}; asm volatile("s_nop 1\n\tv_smfmac_f32_16x16x64_bf16 %0, %1, %2, %3" : "+v"(c) : "v"(a), "v"(w), "v"(idx)); }
DEV void mf_av0(v4f& c, v4u a, v4u b) { asm volatile("v_mfma_f32_16x16x32_bf16 %0, %1, %2, 0" : "=&v"(c) : "a"(a), "v"(b)); }
DEV void mf_vv0(v4f& c, v4u a, v4u b) { asm volatile("v_mfma_f32_16x16x32_bf16 %0, %1, %2, 0" : "=&v"(c) : "v"(a), "v"(b)); }
DEV void mf_av(v4f& c, v4u a, v4u b) { asm volatile("v_mfma_f32_16x16x32_bf16 %0, %1, %2, %0" : "+v"(c) : "a"(a), "v"(b)); }
DEV void mf_va0(v4f& c, v4u a, v4u b) { asm volatile("v_mfma_f32_16x16x32_bf16 %0, %1, %2, 0" : "=&v"(c) : "v"(a), "a"(b)); }
DEV void mf_va(v4f& c, v4u a, v4u b) { asm volatile("v_mfma_f32_16x16x32_bf16 %0, %1, %2, %0" : "+v"(c) : "v"(a), "a"(b)); }
DEV void mf_vv(v4f& c, v4u a, v4u b) { asm volatile("v_mfma_f32_16x16x32_bf16 %0, %1, %2, %0" : "+v"(c) : "v"(a), "v"(b)); }
DEV void mf_vvn(v4f& c, v4u a, v4u b) { asm volatile("s_nop 1\n\tv_mfma_f32_16x16x32_bf16 %0, %1, %2, %0" : "+v"(c) : "v"(a), "v"(b)); }
DEV void settle2(v4f& a, v4f& b) { asm volatile("s_nop 7" : "+v"(a), "+v"(b)); }
DEV void settle3(v4f& a, v4f& b, v4f& c) { asm volatile("s_nop 7" : "+v"(a), "+v"(b), "+v"(c)); }
DEV void late2(v4f& a, v4f& b) { asm volatile("" : "+v"(a), "+v"(b)); }
template <int CTRL> DEV float dpp(float v) { return __uint_as_float(__builtin_amdgcn_update_dpp(0u, __float_as_uint(v), CTRL, 0xf, 0xf, true)); }
#define DPPSTEP(op, ctl) "s_nop 1\n\t" op " %0, %0, %0 " ctl " row_mask:0xf bank_mask:0xf\n\t"
DEV float max16(float m) {
    asm volatile(DPPSTEP("v_max_f32_dpp", "quad_perm:[1,0,3,2]") DPPSTEP("v_max_f32_dpp", "quad_perm:[2,3,0,1]")
                 DPPSTEP("v_max_f32_dpp", "row_half_mirror") DPPSTEP("v_max_f32_dpp", "row_mirror") : "+v"(m));
    return m;
}
DEV float sum16(float m) {
    asm volatile(DPPSTEP("v_add_f32_dpp", "quad_perm:[1,0,3,2]") DPPSTEP("v_add_f32_dpp", "quad_perm:[2,3,0,1]")
                 DPPSTEP("v_add_f32_dpp", "row_half_mirror") DPPSTEP("v_add_f32_dpp", "row_mirror") : "+v"(m));
    return m;
}
DEV void mf16_vv(v4f& c, v2u a, v2u b) { asm volatile("v_mfma_f32_16x16x16_bf16 %0, %1, %2, %0" : "+v"(c) : "v"(a), "v"(b)); }
DEV void late3(v4f& a, v4f& b, v4f& c) { asm volatile("" : "+v"(a), "+v"(b), "+v"(c)); }
DEV void late1(v4f& a) { asm volatile("" : "+v"(a)); }
DEV void settle1(v4f& a) { asm volatile("s_nop 7" : "+v"(a)); }
DEV void lgkm0() { asm volatile("s_waitcnt lgkmcnt(0)" ::: "memory"); }
DEV v4u cat4(v2u a, v2u b) { return __builtin_shufflevector(a, b, 0, 1, 2, 3); }
DEV void st8(rsrc_t r, unsigned off, v2u v) { __builtin_amdgcn_raw_buffer_store_b64(v, r, off, 0, 16); }
DEV v8u cat8(v4u a, v4u b) { return __builtin_shufflevector(a, b, 0, 1, 2, 3, 4, 5, 6, 7); }
DEV v4u lo4(v8u v) { return __builtin_shufflevector(v, v, 0, 1, 2, 3); }
DEV v4u hi4(v8u v) { return __builtin_shufflevector(v, v, 4, 5, 6, 7); }
DEV v4u lds16(const char* p) { return *(const LAS v4u*)(p); }
DEV void sts16(char* p, v4u v) { *(LAS v4u*)(p) = v; }
DEV v2u trread(const char* p) { return __builtin_bit_cast(v2u, __builtin_amdgcn_ds_read_tr16_b64_v4i16((LAS v4s*)(p))); }
DEV void swap32(float& a, float& b) { auto r = __builtin_amdgcn_permlane32_swap(__float_as_uint(a), __float_as_uint(b), false, false); a = __uint_as_float(r[0]); b = __uint_as_float(r[1]); }
DEV void swap16(unsigned& a, unsigned& b) { auto r = __builtin_amdgcn_permlane16_swap(a, b, false, false); a = r[0]; b = r[1]; }

DEV float xr16max(float v) { unsigned a = __float_as_uint(v), b = a; swap16(a, b); return fmaxf(__uint_as_float(a), __uint_as_float(b)); }
DEV float xr16sum(float v) { unsigned a = __float_as_uint(v), b = a; swap16(a, b); return __uint_as_float(a) + __uint_as_float(b); }
#define TS(role, s, k) do {} while (0)
#define TSLANE 0
#define CYDECL
#define CY(k) do {} while (0)
#define CYOUT(role) do {} while (0)
#define WAITOK(x) (x)
DEV bool spin_fail(rsrc_t rs, unsigned& spins) {
    ++spins;
    if (spins > SPIN_CAP) { st4(rs, C_ABORT, 1u); return true; }
    if ((spins & 127u) == 0u) { if (__builtin_amdgcn_readfirstlane(ld4(rs, C_ABORT))) return true; }
    return false;
}
DEV bool wait_groups(rsrc_t rs, unsigned base, int n, unsigned need, int lane, unsigned& seen) {
    if (seen >= need) return true;
    const unsigned off = base + (unsigned)(lane < n ? lane : 0) * 16u;
    for (unsigned spins = 0;;) {
        unsigned m = min4(ld16(rs, off));
        asm volatile("" ::: "memory");
        if (WAITOK(__all(m >= need))) {
            unsigned lo = need;
#pragma unroll
            for (int k = 4; k >= 1; k >>= 1) if (__all(m >= lo + k)) lo += k;
            seen = lo;
            return true;
        }
        __builtin_amdgcn_s_sleep(POLLSLEEP);
        if (spin_fail(rs, spins)) return false;
    }
}
DEV bool wait_word(rsrc_t rs, unsigned off, unsigned need, unsigned& got) {
    for (unsigned spins = 0;;) {
        got = __builtin_amdgcn_readfirstlane(ld4(rs, off));
        asm volatile("" ::: "memory");
        if (WAITOK(got >= need)) return true;
        __builtin_amdgcn_s_sleep(POLLSLEEP);
        if (spin_fail(rs, spins)) return false;
    }
}
DEV bool wait_head(rsrc_t rs, unsigned base, int h, unsigned need, int lane, unsigned& seen) {
    const int j0 = (6 * h) >> 3, j1 = (6 * h + 5) >> 3;
    return wait_groups(rs, base + j0 * 16, j1 - j0 + 1, need, lane, seen);
}
DEV void post(rsrc_t rs, unsigned group, int wave, int lane, unsigned val) {
    drain();
    if (lane == 0) st4(rs, group + wave * 4, val);
}

DEV void dma16(const void* gbase, unsigned voff, unsigned ldsaddr) {
    unsigned keep;
    asm volatile("s_mov_b32 %0, m0\n\ts_mov_b32 m0, %3\n\ts_nop 0\n\tglobal_load_lds_dwordx4 %1, %2\n\ts_mov_b32 m0, %0"
                 : "=&s"(keep) : "v"(voff), "s"(gbase), "s"(ldsaddr) : "memory");
}
DEV void vmwait_n(int n) { asm volatile("s_waitcnt vmcnt(%0)" :: "n"(n) : "memory"); }
template <int N> DEV void vmwait() { asm volatile("s_waitcnt vmcnt(%0)" :: "n"(N) : "memory"); }
template <int ROWB> DEV int stg_f(int row) { return (ROWB == 256 || ROWB == 512) ? ((row & 3) | (((row >> 3) & 1) << 2)) : (((row >> 1) & 1) | (((row >> 3) & 1) << 1)); }
template <int ROWB> DEV int stg_src(int row, int ch) { return ch ^ (stg_f<ROWB>(row) << 1); }
template <int ROWB> DEV v8u tr_frag(const char* tile, int r0, int lane, int colrun) {
    const int g = lane >> 4, q = (lane & 15) >> 2, row = 8 * g + q;
    const char* a = tile + (r0 + row) * ROWB + ((colrun * 2) ^ (stg_f<ROWB>(row) << 5));
    const v2u x0 = trread(a), x1 = trread(a + 4 * ROWB), x2 = trread(a + 32 * ROWB), x3 = trread(a + 36 * ROWB);
    v8u r; r[0] = x0.x; r[1] = x0.y; r[2] = x1.x; r[3] = x1.y; r[4] = x2.x; r[5] = x2.y; r[6] = x3.x; r[7] = x3.y;
    return r;
}
constexpr int STG = 24576;

DEV bool gather_mem(rsrc_t rs, unsigned par0, int s, int lane, int rw, char* memf, v4u (&A)[3]) {
    const unsigned L = L_MEM + (unsigned)(s & 3) * MEM_SLOT, l16 = lane * 16;
    const int k1 = rw + 1, k2 = rw + 2 >= 10 ? rw - 8 : rw + 2;
    const unsigned o0 = rmap(L + rw * 1024, par0), o1 = rmap(L + k1 * 1024, par0), o2 = rmap(L + k2 * 1024, par0);
    for (unsigned spins = 0;;) {
        A[0] = ld16s(rs, l16, o0); A[1] = ld16s(rs, l16, o1); A[2] = ld16s(rs, l16, o2);
        asm volatile("" ::: "memory");
        if (WAITOK(__all(min(min(min4(A[0]), min4(A[1])), min4(A[2])) != 0u))) break;
        if (spin_fail(rs, spins)) return false;
    }
    char* d = memf + lane * 16;
    sts16(d + rw * 1024, A[0]); sts16(d + k1 * 1024, A[1]); sts16(d + k2 * 1024, A[2]);
    sts16(d + (rw + 10) * 1024, A[0]); sts16(d + (k1 + 10) * 1024, A[1]); sts16(d + (k2 + 10) * 1024, A[2]);
    return true;
}
DEV unsigned wm_off(int s, unsigned L, unsigned par) {
    const unsigned sl = s & 3;
    return sl < 2 ? rmap(L_WM + sl * WM_SLOT + L, par & 1u) : PAGE + rmap((sl - 2) * WM_SLOT + L, par >> 1);
}

DEV unsigned wm_base(int s, unsigned par) { return wm_off(s, 0u, par); }
constexpr unsigned P_FIN = 16384;
constexpr int WOFF = 40960;

constexpr int S_PASS[20] = {0, 1, 2, 3, 5, 6, 7, 9, 10, 11, 13, 14, 16, 17, 19, 4, 8, 12, 15, 18};
constexpr int s_pos(int ms) { for (int i = 0; i < 20; ++i) if (S_PASS[i] == ms) return i; return -1; }
constexpr int S_STREAM[15] = {0, 1, 2, 3, 4, 5, 6, 9, 12, 7, 10, 13, 8, 11, 14};

DEV void role_S(const Params& p, rsrc_t rs, int h, char* lds, int t) {
    const int lane = t & 63, wave = __builtin_amdgcn_readfirstlane(t >> 6), n = lane & 15, g = lane >> 4;
    char* const MEMF = lds;
    char* const QK = lds + 20480;
    char* const SPX = lds + 28160;
    char* const ST = lds + 32768;
    char* const WA = lds + WOFF;
    constexpr int SPIN = 31;
    const int rw = (5 * wave + 1) >> 1;
    const int efull = (wave & 1) ? 48 * wave + 16 : 48 * wave, ehalf = (wave & 1) ? 48 * wave : 48 * wave + 32;
    v8u W[45];
    {
        unsigned vo[6], rr[6];
#pragma unroll
        for (int k = 0; k < 6; ++k) {
            const int i = (wave * 6 + k) * 64 + lane, ch = stg_src<384>(i / 24, i % 24);
            vo[k] = (unsigned)((i / 24) * HD * 2 + ch * 16); rr[k] = (5 * (ch / 6) + 1) >> 1;
        }
        const unsigned ldsb = (unsigned)(size_t)(LAS char*)WA + wave * 6144;
        auto issue = [&](int ps) {
            const uint16_t* src = (S_PASS[ps] < 10 ? p.Wq : p.Wk) + h * DH;
#pragma unroll
            for (int k = 0; k < 6; ++k) {
                unsigned ks = rr[k] + S_PASS[ps] % 10; ks = ks >= 10 ? ks - 10 : ks;
                dma16(src, vo[k] + ks * (64 * HD * 2), ldsb + (ps % 5) * STG + k * 1024);
            }
        };
#pragma unroll
        for (int ps = 0; ps < 4; ++ps) issue(ps);
        if (t < 8) *(LAS v4u*)(ST + t * 16) = (v4u){0, 0, 0, 0};
#pragma unroll
        for (int ps = 0; ps < 20; ++ps) {
            if (ps <= 16) vmwait<18>(); else if (ps == 17) vmwait<12>(); else if (ps == 18) vmwait<6>(); else vmwait<0>();
            bar();
            if (ps + 4 < 20) issue(ps + 4);
            const char* tile = WA + (ps % 5) * STG;
            v8u f[3];
#pragma unroll
            for (int tl = 0; tl < 3; ++tl) f[tl] = tr_frag<384>(tile, 0, lane, (tl < 2 ? efull + 16 * tl : ehalf) + 4 * (lane & 3));
            if (ps < 15) {
#pragma unroll
                for (int tl = 0; tl < 3; ++tl) { W[ps * 3 + tl] = f[tl]; if (ps * 3 + tl < SPIN) asm volatile("" : "+a"(W[ps * 3 + tl])); }
            } else {
                bar();
#pragma unroll
                for (int tl = 0; tl < 3; ++tl) {
                    char* d = WA + (ps % 5) * STG + (wave * 3 + tl) * 2048 + lane * 16;
                    sts16(d, lo4(f[tl])); sts16(d + 1024, hi4(f[tl]));
                }
            }
        }
    }
    TS(h, 64, 0);
    unsigned parw;
    if (!wait_word(rs, C_PAR, 1u, parw)) return;
    const unsigned par0 = (parw - 1u) & 1u;
    const int idx = (lane & 8) ? 0xEEEE : 0x4444;
    const int bfull = efull >> 5, bhalf = ehalf >> 5;
    const int gh = 2 * (wave & 1) + (g >> 1);
    const float sc2 = 0.07216878364870322f * 1.4426950408889634f;
    v4u kf[2]; v2u kh[2];
    unsigned seenK = 0;
    auto fetch_kx = [&](int s) -> bool {
        if (!wait_head(rs, C_FK, h, s + 1, lane, seenK)) return false;
        const unsigned b = B_KX + (unsigned)(s * NH + h) * 12288u;
#pragma unroll
        for (int kt = 0; kt < 2; ++kt) {
            kf[kt] = ld16s(rs, lane * 16, b + (kt * 6 + bfull) * 1024);
            kh[kt] = ld8s(rs, (gh * 16 + n) * 16 + (g & 1) * 8, b + (kt * 6 + bhalf) * 1024);
        }
        return true;
    };
    const char* const wlb = WA + wave * 6144 + lane * 16;
    auto wl = [&](int i) -> v8u { const char* d = wlb + (S_STREAM[i] / 3) * STG + (S_STREAM[i] % 3) * 2048; return cat8(lds16(d), lds16(d + 1024)); };
    v8u WL[3];
#pragma unroll
    for (int i = 0; i < 3; ++i) WL[i] = wl(i);
    const int q4 = n >> 2, pp = lane & 3;
    char* const wrAB = QK + ((lane < 32 ? efull : efull + 16) + n) * 40 + (g & 1) * 8;
    char* const wrC = QK + (ehalf + n) * 40 + (g & 1) * 8 + (g >> 1) * 4;
    const char* const trF = QK + (efull + 8 * g + q4) * 40 + pp * 8;
    const char* const trH = QK + (ehalf + 4 * g + q4) * 40 + pp * 8;
    const char* const trAll = QK + (8 * g + q4) * 40 + pp * 8;
    const char* const arot = MEMF + rw * 1024 + lane * 16;
    const int exrow = 2 * wave + (g & 1);
    const char* const exrd = SPX + exrow * 144 + n * 8;
    char* const exwr = SPX + wave * 1152 + (n & 7) * 144 + g * 32;
    const unsigned vexs = (lane < 32 && !(lane & 3)) ? lane * 4 : OOB;
    const unsigned vfin = (g >= 2 && n < 8) ? (g - 2) * 128 + n * 16 : OOB;
    TS(h, 64, 1);
    if (!fetch_kx(0)) return;
    TS(h, 64, 2);
    CYDECL;
    for (int s = 0; s < NSEG; ++s) {
        CY(0);
        TS(h, s, 0);
        v4f aq[3], ak[3];
#pragma unroll
        for (int tl = 0; tl < 3; ++tl) { aq[tl] = (v4f){0, 0, 0, 0}; ak[tl] = (v4f){0, 0, 0, 0}; }
        late3(aq[0], aq[1], aq[2]); late3(ak[0], ak[1], ak[2]);
        v4u A[10];
        int li = 0;
        auto mm = [&](v4f& acc, int ms, int tl) {
            const int ps = s_pos(ms);
            if (ps * 3 + tl < SPIN) smf_a(acc, A[ms % 10], W[ps * 3 + tl], idx);
            else if (ps < 15) smf_v(acc, A[ms % 10], W[ps * 3 + tl], idx);
            else { smf_v(acc, A[ms % 10], WL[li % 3], idx); WL[li % 3] = wl((li + 3) % 15); ++li; SB(); }
        };
        {
            v4u own[3];
            if (!gather_mem(rs, par0, s, lane, rw, MEMF, own)) return;
            A[0] = own[0]; A[1] = own[1]; A[2] = own[2];
        }
        CY(1);
        TS(h, s, 1);
        bar();
        CY(6);
#pragma unroll
        for (int j = 3; j < 10; ++j) A[j] = lds16(arot + j * 1024);
        SB();
#pragma unroll
        for (int j = 0; j < 10; ++j)
#pragma unroll
            for (int tl = 0; tl < 3; ++tl) mm(aq[tl], j, tl);
        CY(2);
        float r[4];
        mm(ak[0], 10, 0); mm(ak[0], 11, 0);
        asm volatile("s_nop 4" : "+v"(aq[0]), "+v"(aq[1]), "+v"(aq[2]));
        mm(ak[0], 12, 0);
        { float a = aq[0][0], b = aq[1][0]; swap32(a, b); r[0] = a + b; } SB();
        mm(ak[0], 13, 0);
        { float a = aq[0][1], b = aq[1][1]; swap32(a, b); r[1] = a + b; } SB();
        mm(ak[0], 14, 0);
        { float a = aq[0][2], b = aq[1][2]; swap32(a, b); r[2] = a + b; } SB();
        mm(ak[0], 15, 0);
        { float a = aq[0][3], b = aq[1][3]; swap32(a, b); r[3] = a + b; } SB();
        mm(ak[0], 16, 0);
        *(LAS v2u*)wrAB = (v2u){pk2(r[0], r[1]), pk2(r[2], r[3])}; SB();
        mm(ak[0], 17, 0);
        { float a = aq[2][0], b = aq[2][2]; swap32(a, b); r[0] = a + b; } SB();
        mm(ak[0], 18, 0);
        { float a = aq[2][1], b = aq[2][3]; swap32(a, b); r[1] = a + b; *(LAS unsigned*)wrC = pk2(r[0], r[1]); } SB();
        mm(ak[0], 19, 0);
        const v4u Ff = cat4(trread(trF), trread(trF + 160)); const v2u Fh = trread(trH); SB();
        mm(ak[1], 10, 1); mm(ak[1], 11, 1); mm(ak[1], 12, 1); mm(ak[1], 13, 1);
        v4f sx0, sx1;
        mf_vv0(sx0, kf[0], Ff); mf_vv0(sx1, kf[1], Ff);
        mm(ak[1], 14, 1); mm(ak[1], 15, 1);
        asm volatile("s_nop 2" ::);
        mf16_vv(sx0, kh[0], Fh); mf16_vv(sx1, kh[1], Fh);
        mm(ak[1], 16, 1); mm(ak[1], 17, 1);
        asm volatile("s_nop 5" : "+v"(sx0), "+v"(sx1));
        if (n < 8) { *(LAS v4f*)exwr = sx0; *(LAS v4f*)(exwr + 16) = sx1; }
        SB();
        mm(ak[1], 18, 1); mm(ak[1], 19, 1);
        bar();
        const v2u e0 = *(const LAS v2u*)exrd, e1 = *(const LAS v2u*)(exrd + 1152), e2 = *(const LAS v2u*)(exrd + 2304), e3 = *(const LAS v2u*)(exrd + 3456); SB();
        mm(ak[2], 10, 2); mm(ak[2], 11, 2);
        const float x0 = (__uint_as_float(e0.x) + __uint_as_float(e1.x)) + (__uint_as_float(e2.x) + __uint_as_float(e3.x));
        const float x1 = (__uint_as_float(e0.y) + __uint_as_float(e1.y)) + (__uint_as_float(e2.y) + __uint_as_float(e3.y));
        const unsigned PL = L_P + (unsigned)(s & 3) * P_SLOT;
        {
            const float m = max16(fmaxf(x0, x1));
            const float ms = m * sc2;
            const float y0 = __builtin_amdgcn_exp2f(x0 * sc2 - ms), y1 = __builtin_amdgcn_exp2f(x1 * sc2 - ms);
            const float yd = __uint_as_float(nz(pk2(y0, y1)));
            if (s + 1 < NSEG) st16s(rs, vexs, rmap(PL + h * 512 + wave * 128, par0),
                                    (v4u){__float_as_uint(yd), __float_as_uint(dpp<0x55>(yd)), __float_as_uint(dpp<0xAA>(yd)), __float_as_uint(dpp<0xFF>(yd))});
            const float l = sum16(y0 + y1);
            if (n == 0) *(LAS v4u*)(ST + exrow * 16) = (v4u){__float_as_uint(m), __float_as_uint(l), (unsigned)(s + 1), 0u};
            SB();
        }
        asm volatile("s_nop 1" : "+v"(ak[0]), "+v"(ak[1]));
        { float a = ak[0][0], b = ak[1][0]; swap32(a, b); r[0] = a + b; } SB();
        mm(ak[2], 12, 2);
        { float a = ak[0][1], b = ak[1][1]; swap32(a, b); r[1] = a + b; } SB();
        mm(ak[2], 13, 2);
        { float a = ak[0][2], b = ak[1][2]; swap32(a, b); r[2] = a + b; } SB();
        mm(ak[2], 14, 2);
        { float a = ak[0][3], b = ak[1][3]; swap32(a, b); r[3] = a + b; } SB();
        mm(ak[2], 15, 2);
        *(LAS v2u*)(wrAB + 16) = (v2u){pk2(r[0], r[1]), pk2(r[2], r[3])}; SB();
        mm(ak[2], 16, 2);
        mm(ak[2], 17, 2); mm(ak[2], 18, 2); mm(ak[2], 19, 2);
        CY(3);
        settle1(ak[2]);
        { float a = ak[2][0], b = ak[2][2]; swap32(a, b); r[0] = a + b; }
        { float a = ak[2][1], b = ak[2][3]; swap32(a, b); r[1] = a + b; *(LAS unsigned*)(wrC + 16) = pk2(r[0], r[1]); } SB();
        bar();
        CY(4);
        TS(h, s, 2);
        if (wave == 0) {
            if (s + 1 < NSEG) {
                v4u F[6];
#pragma unroll
                for (int b = 0; b < 6; ++b) F[b] = cat4(trread(trAll + b * 1280), trread(trAll + b * 1280 + 160));
                SB();
                v4f sm;
                mf_vv0(sm, F[0], F[0]);
#pragma unroll
                for (int b = 1; b < 4; ++b) mf_vv(sm, F[b], F[b]);
                v4u ml = *(const volatile LAS v4u*)(ST + (n & 7) * 16);
                SB();
                mf_vv(sm, F[4], F[4]); mf_vv(sm, F[5], F[5]);
                settle1(sm);
                const float mm0 = xr16max(fmaxf(fmaxf(sm[0], sm[1]), fmaxf(sm[2], sm[3])));
                for (unsigned spins = 0; !__all(ml.z == (unsigned)(s + 1)) && spins < (1u << 20); ++spins) ml = *(const volatile LAS v4u*)(ST + (n & 7) * 16);
                const float mx = __uint_as_float(ml.x);
                const float m = fmaxf(mm0, mx);
                const float ms = m * sc2;
                const float al = __builtin_amdgcn_exp2f(mx * sc2 - ms);
                float e[4];
#pragma unroll
                for (int i = 0; i < 4; ++i) e[i] = __builtin_amdgcn_exp2f(sm[i] * sc2 - ms);
                const float l = xr16sum((e[0] + e[1]) + (e[2] + e[3])) + al * __uint_as_float(ml.y);
                const float inv = __builtin_amdgcn_rcpf(l);
                unsigned a0 = nz(pk2(e[0] * inv, e[1] * inv)), a1 = nz(pk2(e[2] * inv, e[3] * inv)), b0 = a0, b1 = a1;
                swap16(a0, b0); swap16(a1, b1);
                const unsigned sc = nz(__float_as_uint(al * inv));
                const unsigned FL = rmap(PL + P_FIN + h * 256, par0);
                st16s(rs, vfin, FL, g == 2 ? (v4u){a0, a1, b0, b1} : (v4u){sc, sc, sc, sc});
            }
            if (s > 0) {
                const unsigned ZL = L_P + (unsigned)((s - 1) & 3) * P_SLOT;
                if (lane < 32) st16s(rs, lane * 16, rmap(ZL + h * 512, par0), (v4u){0, 0, 0, 0});
                if (lane < 16) st16s(rs, lane * 16, rmap(ZL + P_FIN + h * 256, par0), (v4u){0, 0, 0, 0});
            }
        } else if (wave == 3) {
#pragma unroll
            for (int b = 0; b < 6; ++b) {
                const v4u F = cat4(trread(trAll + b * 1280), trread(trAll + b * 1280 + 160));
                if (n >= 8) st16s(rs, (g * 8 + n - 8) * 16, B_KM + (unsigned)(s * NH + h) * 3072u + b * 512, F);
            }
            post(rs, C_FKM + h * 128, 0, lane, s + 1);
        }
        CY(5);
        TS(h, s, 3);
        if (s + 1 < NSEG) { if (!fetch_kx(s + 1)) return; }
    }
    CYOUT(h);
}

struct VPass { int kind, id, buf, res; };
constexpr int VR0 = WOFF, VR1 = WOFF + 24576, VR2 = WOFF + 49152, VR3 = WOFF + 73728, VR4 = VR3 + 16384, VR5 = VR4 + 16384, VE0 = 0, VE1 = 16384;
constexpr VPass V_PASS[25] = {
    {0, 0, VR0, 0}, {0, 1, VR1, 0}, {0, 2, VR2, 0}, {0, 3, VR3, 0}, {0, 5, VR0, 0}, {0, 7, VR1, 0}, {0, 9, VR2, 0},
    {1, 0, VR5, 0}, {1, 1, VE0, 0}, {1, 2, VE1, 0}, {0, 4, VR0, 1}, {0, 6, VR1, 1}, {0, 8, VR2, 1},
    {1, 3, VR3, 0}, {1, 4, VR4, 0}, {1, 5, VR5, 0}, {1, 6, VE0, 0}, {1, 7, VE1, 0}, {1, 8, VR3, 0}, {1, 9, VR4, 0}, {1, 10, VR5, 0}, {1, 11, VE0, 0},
    {1, 12, VR3, 1}, {1, 13, VR4, 1}, {1, 14, VR5, 1}};
constexpr bool v_overlap(int a, int b) {
    const int a0 = V_PASS[a].buf, a1 = a0 + (V_PASS[a].kind ? 16384 : 24576), b0 = V_PASS[b].buf, b1 = b0 + (V_PASS[b].kind ? 16384 : 24576);
    return a0 < b1 && b0 < a1;
}
struct VTab { int issued[26], wait[25]; };
constexpr VTab v_tab() {
    VTab tb{};
    int j = 0;
    for (int cur = -1; cur < 25; ++cur) {
        for (; j < 25 && j <= cur + 6; ++j) {
            bool ok = true;
            for (int k = cur < 0 ? 0 : cur; k < j; ++k) if (v_overlap(k, j)) ok = false;
            if (!ok) break;
        }
        tb.issued[cur + 1] = j;
    }
    for (int i = 0; i < 25; ++i) { int c = 0; for (int k = i + 1; k < tb.issued[i]; ++k) c += V_PASS[k].kind ? 4 : 6; tb.wait[i] = c; }
    return tb;
}
constexpr VTab V_TAB = v_tab();
static_assert(V_TAB.issued[25] == 25, "load schedule incomplete");
constexpr int v_reg(int kind, int id, int j) {
    if (kind == 0) {
        if (id == 4 || id == 6 || id == 8) return -1 - (j < 2 ? (id - 4) + j : 6 + (id - 4) / 2);
        const int o = id < 4 ? id : 4 + (id - 5) / 2;
        return o * 3 + j;
    }
    if (id >= 12) return -1 - (9 + (id - 12) * 2 + j);
    return 21 + id * 2 + j;
}
constexpr int v_lds(int i) {
    if (i < 6) return (i / 2 == 0 ? VR0 : i / 2 == 1 ? VR1 : VR2) + (i & 1) * 2048;
    if (i < 9) return (i == 6 ? VR0 : i == 7 ? VR1 : VR2) + 4096;
    return ((i - 9) / 2 == 0 ? VR3 : (i - 9) / 2 == 1 ? VR4 : VR5) + ((i - 9) & 1) * 2048;
}

DEV void role_V(const Params& p, rsrc_t rs, int h, char* lds, int t) {
    const int lane = t & 63, wave = __builtin_amdgcn_readfirstlane(t >> 6), n = lane & 15, g = lane >> 4;
    char* const MEMF = lds;
    char* const VI = lds + 20480;
    constexpr int VPIN = 31;
    const int rw = (5 * wave + 1) >> 1;
    v8u W[45];
    {
        unsigned vo[6], rr[6], vo2[4];
#pragma unroll
        for (int k = 0; k < 6; ++k) {
            const int i = (wave * 6 + k) * 64 + lane, ch = stg_src<384>(i / 24, i % 24);
            vo[k] = (unsigned)((i / 24) * HD * 2 + ch * 16); rr[k] = (5 * (ch / 6) + 1) >> 1;
        }
#pragma unroll
        for (int k = 0; k < 4; ++k) { const int i = (wave * 4 + k) * 64 + lane, r = i >> 4, ch = stg_src<256>(r, i & 15); vo2[k] = (unsigned)(r * D * 2 + (ch >> 2) * 320 + (ch & 3) * 16); }
        const unsigned ldsl = (unsigned)(size_t)(LAS char*)lds;
        auto issue = [&](auto ic) {
            constexpr VPass P = V_PASS[decltype(ic)::value];
            if constexpr (P.kind == 0) {
                const uint16_t* src = p.Wv + h * DH;
#pragma unroll
                for (int k = 0; k < 6; ++k) {
                    unsigned ks = rr[k] + P.id; ks = ks >= 10 ? ks - 10 : ks;
                    dma16(src, vo[k] + ks * (64 * HD * 2), ldsl + P.buf + wave * 6144 + k * 1024);
                }
            } else {
                const uint16_t* src = p.Wo + ((size_t)h * DH + (P.id / 5) * 64) * D + (P.id % 5) * 32;
#pragma unroll
                for (int k = 0; k < 4; ++k) dma16(src, vo2[k], ldsl + P.buf + wave * 4096 + k * 1024);
            }
        };
        sfor<0, V_TAB.issued[0]>(issue);
        sfor<0, 25>([&](auto ic) {
            constexpr int ps = decltype(ic)::value;
            constexpr VPass P = V_PASS[ps];
            constexpr int nf = P.kind ? 2 : 3;
            vmwait<V_TAB.wait[ps]>();
            bar();
            sfor<V_TAB.issued[ps], V_TAB.issued[ps + 1]>(issue);
            const char* tile = lds + P.buf;
            v8u f[nf];
#pragma unroll
            for (int j = 0; j < nf; ++j)
                f[j] = P.kind ? tr_frag<256>(tile, 0, lane, 32 * wave + 16 * j + 4 * (lane & 3)) : tr_frag<384>(tile, 0, lane, 48 * wave + 16 * j + 4 * (lane & 3));
            if constexpr (!P.res) {
#pragma unroll
                for (int j = 0; j < nf; ++j) { const int ri = v_reg(P.kind, P.id, j); W[ri] = f[j]; if (ri < VPIN) asm volatile("" : "+a"(W[ri])); }
            } else {
                bar();
#pragma unroll
                for (int j = 0; j < nf; ++j) { char* d = lds + P.buf + (wave * nf + j) * 2048 + lane * 16; sts16(d, lo4(f[j])); sts16(d + 1024, hi4(f[j])); }
            }
        });
        bar();
    }
    unsigned parw;
    if (!wait_word(rs, C_PAR, 1u, parw)) return;
    const unsigned par = (parw - 1u) & 3u, par0 = par & 1u;
    const int idx = (lane & 8) ? 0xEEEE : 0x4444;
    const char* const gb3 = lds + wave * 6144 + lane * 16;
    const char* const gb2 = lds + wave * 4096 + lane * 16;
    auto gl = [&](int i) -> v8u { const char* d = (i < 9 ? gb3 : gb2) + v_lds(i); return cat8(lds16(d), lds16(d + 1024)); };
    v8u G[5];
#pragma unroll
    for (int i = 0; i < 5; ++i) G[i] = gl(i);
    const int q4 = n >> 2, pp = lane & 3;
    char* const wrAB = VI + (48 * wave + (lane < 32 ? 0 : 16) + n) * 16 + (g & 1) * 8;
    char* const wrC = VI + (48 * wave + 32 + n) * 16 + (g & 1) * 8 + (g >> 1) * 4;
    const char* const trV = VI + (16 * g + 4 * (q4 >> 1) + (q4 & 1) + 2 * (pp >> 1)) * 16 + (pp & 1) * 8;
    const char* const arot = MEMF + rw * 1024 + lane * 16;
    const unsigned vw4 = (2 * (g & 1) + (g >> 1)) * 16384 + n * 16, vw2 = (g >> 1) * 16384 + n * 16;
    TS(32 + h, 64, 1);
    CYDECL;
    for (int s = 0; s < NSEG; ++s) {
        CY(0);
        TS(32 + h, s, 0);
        v4f av[3], ac[2][2];
#pragma unroll
        for (int tl = 0; tl < 3; ++tl) av[tl] = (v4f){0, 0, 0, 0};
        late3(av[0], av[1], av[2]);
        const unsigned wmb = wm_base(s, par) + (unsigned)(wave * 10) * 16384u + h * 256;
        auto mm = [&](v4f& acc, v4u a, int kind, int id, int j) {
            const int ri = v_reg(kind, id, j);
            if (ri >= VPIN) smf_v(acc, a, W[ri], idx);
            else if (ri >= 0) smf_a(acc, a, W[ri], idx);
            else { const int li = -1 - ri; smf_v(acc, a, G[li % 5], idx); G[li % 5] = gl((li + 5) % 15); SB(); }
        };
        v4u A[10];
        {
            v4u own[3];
            if (!gather_mem(rs, par0, s, lane, rw, MEMF, own)) return;
            A[0] = own[0]; A[1] = own[1]; A[2] = own[2];
        }
        CY(1);
        TS(32 + h, s, 1);
        bar();
        CY(6);
#pragma unroll
        for (int j = 3; j < 10; ++j) A[j] = lds16(arot + j * 1024);
        SB();
#pragma unroll
        for (int j = 0; j < 3; ++j)
#pragma unroll
            for (int tl = 0; tl < 3; ++tl) mm(av[tl], A[j], 0, j, tl);
#pragma unroll
        for (int j = 3; j < 10; ++j) { mm(av[0], A[j], 0, j, 0); mm(av[1], A[j], 0, j, 1); }
        float r[4];
        mm(av[2], A[3], 0, 3, 2); mm(av[2], A[4], 0, 4, 2);
        ac[0][0] = (v4f){0, 0, 0, 0}; ac[0][1] = (v4f){0, 0, 0, 0}; late2(ac[0][0], ac[0][1]);
        asm volatile("s_nop 1" : "+v"(av[0]), "+v"(av[1]));
        mm(av[2], A[5], 0, 5, 2);
        { float a = av[0][0], b = av[1][0]; swap32(a, b); r[0] = a + b; } SB();
        mm(av[2], A[6], 0, 6, 2);
        { float a = av[0][1], b = av[1][1]; swap32(a, b); r[1] = a + b; } SB();
        mm(av[2], A[7], 0, 7, 2);
        { float a = av[0][2], b = av[1][2]; swap32(a, b); r[2] = a + b; } SB();
        mm(av[2], A[8], 0, 8, 2);
        { float a = av[0][3], b = av[1][3]; swap32(a, b); r[3] = a + b; } SB();
        mm(av[2], A[9], 0, 9, 2);
        *(LAS v2u*)wrAB = (v2u){pk2(r[0], r[1]), pk2(r[2], r[3])};
        CY(2);
        ac[1][0] = (v4f){0, 0, 0, 0}; ac[1][1] = (v4f){0, 0, 0, 0}; late2(ac[1][0], ac[1][1]);
        asm volatile("s_nop 2" : "+v"(av[2]));
        { float a = av[2][0], b = av[2][2]; swap32(a, b); r[0] = a + b; }
        { float a = av[2][1], b = av[2][3]; swap32(a, b); r[1] = a + b; *(LAS unsigned*)wrC = pk2(r[0], r[1]); }
        bar();
        CY(3);
        v4u VA[3];
#pragma unroll
        for (int ks = 0; ks < 3; ++ks) VA[ks] = cat4(trread(trV + ks * 1024), trread(trV + ks * 1024 + 128));
        unsigned pkd[5][2];
        SB();
        auto half = [&](int pr, int lo) {
            v4f& a0 = ac[pr & 1][0]; v4f& a1 = ac[pr & 1][1];
            float c0, c1;
            { float a = a0[lo], b = a1[lo]; swap32(a, b); c0 = a + b; }
            { float a = a0[lo + 1], b = a1[lo + 1]; swap32(a, b); c1 = a + b; }
            pkd[pr][lo >> 1] = nz(pk2(c0, c1));
            if (lo == 2 && pr < 3) { a0 = (v4f){0, 0, 0, 0}; a1 = a0; late2(a0, a1); }
            SB();
        };
        auto publish = [&](int q) {
            unsigned x0 = pkd[2 * q][0], x1 = pkd[2 * q][1], y0 = pkd[q < 2 ? 2 * q + 1 : 4][0], y1 = pkd[q < 2 ? 2 * q + 1 : 4][1];
            swap16(x0, y0); swap16(x1, y1);
            if (q < 2 || !(g & 1)) st16s(rs, q < 2 ? vw4 : vw2, wmb + (unsigned)(4 * q) * 16384u, (v4u){x0, x1, y0, y1});
            SB();
        };
#pragma unroll
        for (int pr = 0; pr < 5; ++pr) {
            v4f& a0 = ac[pr & 1][0]; v4f& a1 = ac[pr & 1][1];
            mm(a0, VA[0], 1, pr, 0); mm(a1, VA[0], 1, pr, 1);
            if (pr > 0) { asm volatile("s_nop 5" : "+v"(ac[(pr - 1) & 1][0]), "+v"(ac[(pr - 1) & 1][1])); half(pr - 1, 0); }
            mm(a0, VA[1], 1, 5 + pr, 0); mm(a1, VA[1], 1, 5 + pr, 1);
            if (pr > 0) half(pr - 1, 2);
            mm(a0, VA[2], 1, 10 + pr, 0); mm(a1, VA[2], 1, 10 + pr, 1);
            if (pr == 2) publish(0);
            if (pr == 4) publish(1);
        }
        asm volatile("s_nop 2" : "+v"(ac[0][0]), "+v"(ac[0][1]));
        half(4, 0); half(4, 2);
        publish(2);
        CY(4);
        TS(32 + h, s, 2);
        if (s > 0) {
            const unsigned zb = wm_base(s - 1, par) + (unsigned)(wave * 10) * 16384u + h * 256;
#pragma unroll
            for (int q = 0; q < 3; ++q)
                if (q < 2 || !(g & 1)) st16s(rs, q < 2 ? vw4 : vw2, zb + (unsigned)(4 * q) * 16384u, (v4u){0, 0, 0, 0});
        }
    }
    CYOUT(32 + h);
}

DEV void role_R(const Params& p, rsrc_t rs, int c, char* lds, int t, unsigned long long litek_) {
    const int lane = t & 63, wave = __builtin_amdgcn_readfirstlane(t >> 6), n = lane & 15, g = lane >> 4;
    char* const RPB = lds;
    unsigned parw;
    if (!wait_word(rs, C_PAR, 1u, parw)) return;
    const unsigned par = (parw - 1u) & 3u, par0 = par & 1u;
    if (t < 16) {
        const int r = t & 7, hf = t >> 3;
        const unsigned* src = (const unsigned*)(p.mem0 + r * D + 16 * c);
        const v4u o = {src[hf], src[hf + 2], src[hf + 4], src[hf + 6]};
        st16(rs, rmap(L_MEM + c * 256 + t * 16, par0), nz4(o));
    }
    const unsigned vpub = lane < 16 ? lane * 16 : OOB;
    const unsigned vex = (n >> 3) * 512 + (n & 7) * 64 + g * 16, vsc = (n >> 3) * 256 + 128 + (n & 7) * 16, vpm = (4 * (n >> 3) + g) * 256 + (n & 7) * 16;
    unsigned seenU = 0;
    CYDECL;
    for (int s = 0; s < NSEG; ++s) {
        CY(0);
        TS(64 + c, s, 0);
        v4u z[8];
        if (s + 1 < NSEG) {
            if (!wait_groups(rs, C_FU + wave * 128, 8, s + 1, lane, seenU)) return;
#pragma unroll
            for (int i = 0; i < 8; ++i) z[i] = ld16s(rs, lane * 16, B_ZX + (unsigned)((s * 40 + c) * NH + wave * 8 + i) * 1024u);
        }
        const unsigned LP = L_P + (unsigned)(s & 3) * P_SLOT;
        const unsigned ox = rmap(LP + wave * 4096, par0), of = rmap(LP + P_FIN + wave * 2048, par0), ow = wm_base(s, par) + c * 16384 + wave * 2048;
        CY(1);
        TS(64 + c, s, 1);
        v4u pm, w0, w1; unsigned sc[4];
        auto late_loads = [&]() {
            w0 = ld16s(rs, lane * 16, ow); w1 = ld16s(rs, lane * 16 + 1024, ow); pm = ld16s(rs, vpm, of);
#pragma unroll
            for (int i = 0; i < 4; ++i) sc[i] = ld4s(rs, vsc + i * 512, of);
        };
        auto late_ok = [&]() -> bool { return WAITOK(__all(min(min(min(min4(w0), min4(w1)), min4(pm)), min(min(sc[0], sc[1]), min(sc[2], sc[3]))) != 0u)); };
        if (s + 1 < NSEG) {
            v4u ex[4];
            for (unsigned spins = 0;;) {
#pragma unroll
                for (int i = 0; i < 4; ++i) ex[i] = ld16s(rs, vex + i * 1024, ox);
                late_loads();
                asm volatile("" ::: "memory");
                if (spins == 0 && s > 0 && lane == 0) { vmwait<11>(); st4(rs, C_FW + c * 16 + wave * 4, s); }
                if (WAITOK(__all(min(min(min4(ex[0]), min4(ex[1])), min(min4(ex[2]), min4(ex[3]))) != 0u))) break;
                if (spin_fail(rs, spins)) return;
            }
            CY(6);
            v4f xa[4], xb[4];
#pragma unroll
            for (int i = 0; i < 4; ++i) { mf_vv0(xa[i], z[2 * i], ex[i]); mf_vv0(xb[i], z[2 * i + 1], ex[i]); }
            bool ok = late_ok();
            if (!ok) late_loads();
            SB();
            v4f X[4];
            settle2(xa[3], xb[3]); late3(xa[0], xa[1], xa[2]); late3(xb[0], xb[1], xb[2]);
#pragma unroll
            for (int i = 0; i < 4; ++i) X[i] = n < 8 ? xa[i] : xb[i];
            CY(2);
            for (unsigned spins = 0; !ok;) {
                asm volatile("" ::: "memory");
                ok = late_ok();
                if (ok) break;
                if (spin_fail(rs, spins)) return;
                late_loads();
            }
            CY(3);
            TS(64 + c, s, 2);
            v4f aA, aB;
            mf_vv0(aA, w0, pm); mf_vv0(aB, w1, pm);
            v4f Y = X[0] * __uint_as_float(sc[0]);
#pragma unroll
            for (int i = 1; i < 4; ++i) Y += X[i] * __uint_as_float(sc[i]);
            asm volatile("s_nop 3" : "+v"(Y), "+v"(aA), "+v"(aB));
            char* RP = RPB + (s & 1) * 2048;
            {
                float o[4];
#pragma unroll
                for (int i = 0; i < 4; ++i) o[i] = (aA[i] + Y[i]) + dpp<0x128>(aB[i] + Y[i]);
                if (n < 8) {
                    *(LAS v2u*)(RP + wave * 512 + n * 32 + g * 8) = (v2u){__float_as_uint(o[0]), __float_as_uint(o[1])};
                    *(LAS v2u*)(RP + wave * 512 + (n + 8) * 32 + g * 8) = (v2u){__float_as_uint(o[2]), __float_as_uint(o[3])};
                }
            }
            CY(7);
            bar();
            CY(4);
            if (wave == 0) {
                v4f u = *(const LAS v4f*)(RP + g * 512 + n * 32), v = *(const LAS v4f*)(RP + g * 512 + n * 32 + 16);
                float q[4];
#pragma unroll
                for (int i = 0; i < 4; ++i) { float a = u[i], b = v[i]; swap32(a, b); q[i] = a + b; }
                unsigned a0 = __float_as_uint(q[0]), b0 = __float_as_uint(q[2]), a1 = __float_as_uint(q[1]), b1 = __float_as_uint(q[3]);
                swap16(a0, b0); swap16(a1, b1);
                const unsigned dw = nz(pk2(__uint_as_float(a0) + __uint_as_float(b0), __uint_as_float(a1) + __uint_as_float(b1)));
                unsigned d0 = dw, d1 = dw;
                swap16(d0, d1);
                float f0 = __uint_as_float(d0), f2 = f0, f1 = __uint_as_float(d1), f3 = f1;
                swap32(f0, f2); swap32(f1, f3);
                st16s(rs, vpub, rmap(L_MEM + (unsigned)((s + 1) & 3) * MEM_SLOT + c * 256, par0), (v4u){__float_as_uint(f0), __float_as_uint(f1), __float_as_uint(f2), __float_as_uint(f3)});
                CY(5);
                TS(64 + c, s, 3);
            } else if (wave == 1 && lane < 16) st16s(rs, lane * 16, rmap(L_MEM + (unsigned)(s & 3) * MEM_SLOT + c * 256, par0), (v4u){0, 0, 0, 0});
        } else {
            for (unsigned spins = 0;;) {
                w0 = ld16s(rs, lane * 16, ow); w1 = ld16s(rs, lane * 16 + 1024, ow);
                asm volatile("" ::: "memory");
                if (WAITOK(__all(min(min4(w0), min4(w1)) != 0u))) break;
                if (spin_fail(rs, spins)) return;
            }
        }
        st16s(rs, lane * 16, B_ZM + (unsigned)(s * 40 + c) * 8192u + wave * 2048, w0);
        st16s(rs, lane * 16 + 1024, B_ZM + (unsigned)(s * 40 + c) * 8192u + wave * 2048, w1);
        if (s + 2 >= NSEG) post(rs, C_FW + c * 16, wave, lane, s + 1);
    }
    CYOUT(64 + c);
}

DEV void role_U(const Params& p, rsrc_t rs, int h, char* lds, int t) {
    const int lane = t & 63, wave = __builtin_amdgcn_readfirstlane(t >> 6);
    v4u W[60];
    {
        unsigned vo2[4];
#pragma unroll
        for (int k = 0; k < 4; ++k) { const int i = (wave * 4 + k) * 64 + lane, r = i >> 4, ch = stg_src<256>(r, i & 15); vo2[k] = (unsigned)(r * D * 2 + (ch >> 2) * 320 + (ch & 3) * 16); }
        const unsigned ldsl = (unsigned)(size_t)(LAS char*)lds + wave * 4096;
        auto issue = [&](int q) {
            const uint16_t* src = p.Wo + ((size_t)h * DH + (q / 5) * 64) * D + (q % 5) * 32;
#pragma unroll
            for (int k = 0; k < 4; ++k) dma16(src, vo2[k], ldsl + (q % 8) * 16384 + k * 1024);
        };
#pragma unroll
        for (int q = 0; q < 7; ++q) issue(q);
#pragma unroll
        for (int q = 0; q < 15; ++q) {
            vmwait_n(4 * (q + 6 < 15 ? 6 : 14 - q));
            bar();
            if (q + 7 < 15) issue(q + 7);
#pragma unroll
            for (int j = 0; j < 2; ++j) {
                const v8u f = tr_frag<256>(lds + (q % 8) * 16384, 0, lane, 32 * wave + 16 * j + 4 * (lane & 3));
                const int wi = ((q % 5) * 2 + j) * 6 + (q / 5) * 2;
                W[wi] = lo4(f); W[wi + 1] = hi4(f);
                asm volatile("" : "+a"(W[wi]), "+a"(W[wi + 1]));
            }
        }
    }
    v4u nx[12];
    unsigned seenX = 0;
    auto fetch = [&](int s) -> bool {
        if (!wait_head(rs, C_FX, h, s + 1, lane, seenX)) return false;
#pragma unroll
        for (int k = 0; k < 12; ++k) nx[k] = ld16s(rs, lane * 16, B_VX + (unsigned)(s * NH + h) * 12288u + k * 1024);
        return true;
    };
    if (!fetch(0)) return;
    for (int s = 0; s < NSEG; ++s) {
        TS(176 + h, s, 0);
        v4u vx[12];
#pragma unroll
        for (int k = 0; k < 12; ++k) { vx[k] = nx[k]; asm volatile("" : "+v"(vx[k])); }
        const bool pre = s + 1 < NSEG && seenX >= (unsigned)(s + 2);
        if (pre) { if (!fetch(s + 1)) return; }
        TS(176 + h, s, 1);
        v4f ac[2][2];
        SB();
#pragma unroll
        for (int tl = 0; tl <= 10; ++tl) {
            if (tl < 10) {
                v4f& a0 = ac[tl & 1][0]; v4f& a1 = ac[tl & 1][1];
                mf_va0(a0, vx[0], W[tl * 6]); mf_va0(a1, vx[6], W[tl * 6]);
#pragma unroll
                for (int k = 1; k < 6; ++k) { mf_va(a0, vx[k], W[tl * 6 + k]); mf_va(a1, vx[6 + k], W[tl * 6 + k]); }
            }
            if (tl > 0) {
                v4f& a0 = ac[(tl - 1) & 1][0]; v4f& a1 = ac[(tl - 1) & 1][1];
                if (tl == 10) settle2(a0, a1); else late2(a0, a1);
                const v4u o = {pk2(a0[0], a0[1]), pk2(a0[2], a0[3]), pk2(a1[0], a1[1]), pk2(a1[2], a1[3])};
                st16s(rs, lane * 16, B_ZX + (unsigned)((s * 40 + wave * 10 + tl - 1) * NH + h) * 1024u, o);
                SB();
            }
            if (tl == 5 && s > 8) { vmwait<5>(); if (lane == 0) st4(rs, C_FU + h * 16 + wave * 4, s); SB(); }
        }
        TS(176 + h, s, 2);
        if (s <= 8 || s + 1 == NSEG) post(rs, C_FU + h * 16, wave, lane, s + 1);
        if (!pre && s + 1 < NSEG) { if (!fetch(s + 1)) return; }
    }
}

template <int LD> DEV void load_proj_weights(const uint16_t* wbase, char* lds, int lane, int wave, v4u (&W)[60]) {
    unsigned vo[6];
#pragma unroll
    for (int k = 0; k < 6; ++k) { const int i = (wave * 6 + k) * 64 + lane; vo[k] = (unsigned)((i / 24) * LD * 2 + stg_src<384>(i / 24, i % 24) * 16); }
    const unsigned ldsb = (unsigned)(size_t)(LAS char*)lds + wave * 6144;
    auto issue = [&](int ps) {
        const uint16_t* src = wbase + (size_t)ps * 64 * LD;
#pragma unroll
        for (int k = 0; k < 6; ++k) dma16(src, vo[k], ldsb + (ps % 5) * STG + k * 1024);
    };
    const int pp = lane & 3;
    const int efull = (wave & 1) ? 48 * wave + 16 : 48 * wave, ehalf = (wave & 1) ? 48 * wave : 48 * wave + 32;
#pragma unroll
    for (int ps = 0; ps < 4; ++ps) issue(ps);
#pragma unroll
    for (int ps = 0; ps < 10; ++ps) {
        if (ps <= 6) vmwait<18>(); else if (ps == 7) vmwait<12>(); else if (ps == 8) vmwait<6>(); else vmwait<0>();
        bar();
        if (ps + 4 < 10) issue(ps + 4);
        const char* tile = lds + (ps % 5) * STG;
#pragma unroll
        for (int tl = 0; tl < 3; ++tl) {
            const v8u f = tr_frag<384>(tile, 0, lane, tl == 0 ? efull + 8 * pp : tl == 1 ? efull + 8 * pp + 4 : ehalf + 4 * pp);
            W[ps * 6 + tl] = lo4(f); W[ps * 6 + 3 + tl] = hi4(f);
            asm volatile("" : "+a"(W[ps * 6 + tl]), "+a"(W[ps * 6 + 3 + tl]));
        }
    }
    bar();
}
DEV void proj_mfma(const char* AF, int lane, const v4u (&W)[60], v4f (&acc)[3][2]) {
    v4u b[3][2];
    const char* const a0 = AF + (lane ^ (lane >> 4)) * 16;
    const char* const a1 = AF + (lane ^ (lane >> 4) ^ 4) * 16;
#pragma unroll
    for (int kk = 0; kk < 2; ++kk) { b[kk][0] = lds16((kk & 1 ? a1 : a0) + kk * 1024); b[kk][1] = lds16((kk & 1 ? a1 : a0) + (20 + kk) * 1024); }
#pragma unroll
    for (int kk = 0; kk < 20; ++kk) {
        if (kk + 2 < 20) { b[(kk + 2) % 3][0] = lds16((kk & 1 ? a1 : a0) + (kk + 2) * 1024); b[(kk + 2) % 3][1] = lds16((kk & 1 ? a1 : a0) + (22 + kk) * 1024); }
        SB();
#pragma unroll
        for (int tl = 0; tl < 3; ++tl) {
            if (kk == 0) { mf_av0(acc[tl][0], W[tl], b[0][0]); mf_av0(acc[tl][1], W[tl], b[0][1]); }
            else { mf_av(acc[tl][0], W[kk * 3 + tl], b[kk % 3][0]); mf_av(acc[tl][1], W[kk * 3 + tl], b[kk % 3][1]); }
        }
        SB();
    }
    settle3(acc[0][0], acc[1][0], acc[2][0]); settle3(acc[0][1], acc[1][1], acc[2][1]);
}
DEV void proj_store(rsrc_t rs, unsigned base, int lane, int wave, const v4f (&acc)[3][2]) {
    const int n = lane & 15, g = lane >> 4;
    const int bfull = (wave & 1) ? (3 * wave + 1) / 2 : (3 * wave) / 2, bhalf = (wave & 1) ? (3 * wave - 1) / 2 : (3 * wave) / 2 + 1;
#pragma unroll
    for (int rt = 0; rt < 2; ++rt) {
        const v4u o = {pk2(acc[0][rt][0], acc[0][rt][1]), pk2(acc[0][rt][2], acc[0][rt][3]), pk2(acc[1][rt][0], acc[1][rt][1]), pk2(acc[1][rt][2], acc[1][rt][3])};
        st16(rs, base + (rt * 6 + bfull) * 1024 + lane * 16, o);
    }
    unsigned x0 = pk2(acc[2][0][0], acc[2][0][1]), x1 = pk2(acc[2][0][2], acc[2][0][3]), y0 = pk2(acc[2][1][0], acc[2][1][1]), y1 = pk2(acc[2][1][2], acc[2][1][3]);
    swap16(x0, y0); swap16(x1, y1);
    const v4u o = {x0, x1, y0, y1};
    st16(rs, base + ((g & 1) * 6 + bhalf) * 1024 + ((2 * (wave & 1) + (g >> 1)) * 16 + n) * 16, o);
}
DEV bool fetch_hf(rsrc_t rs, int s, int t, unsigned long long& hmask, v4u (&pf)[10]) {
    for (unsigned spins = 0; !((hmask >> s) & 1ull);) {
        hmask = __ballot(ld4(rs, C_FH + (t & 63) * 4) != 0u);
        asm volatile("" ::: "memory");
        if (WAITOK((hmask >> s) & 1ull)) break;
        __builtin_amdgcn_s_sleep(POLLSLEEP);
        if (spin_fail(rs, spins)) return false;
    }
#pragma unroll
    for (int k = 0; k < 10; ++k) pf[k] = ld16s(rs, t * 16, B_HF + (unsigned)s * 40960u + k * 4096);
    return true;
}
DEV void put_hf(char* AF, int t, const v4u (&pf)[10]) {
#pragma unroll
    for (int k = 0; k < 10; ++k) sts16(AF + (k * 256 + t) * 16, pf[k]);
}

DEV void role_KX(const Params& p, rsrc_t rs, int j, char* lds, int t) {
    const int lane = t & 63, wave = __builtin_amdgcn_readfirstlane(t >> 6);
    const int jj = j < 24 ? j : j - 24;
    v4u W[80];
    {
        const uint16_t* wbase = (j < 24 ? p.Wk : p.Wv) + jj * 256;
        unsigned vo[8];
#pragma unroll
        for (int k = 0; k < 8; ++k) { const int i = (wave * 8 + k) * 64 + lane; vo[k] = (unsigned)((i >> 5) * HD * 2 + stg_src<512>(i >> 5, i & 31) * 16); }
        const unsigned ldsb = (unsigned)(size_t)(LAS char*)lds + wave * 8192;
        auto issue = [&](int ps) {
            const uint16_t* src = wbase + (size_t)ps * 64 * HD;
#pragma unroll
            for (int k = 0; k < 8; ++k) dma16(src, vo[k], ldsb + (ps % 5) * 32768 + k * 1024);
        };
#pragma unroll
        for (int ps = 0; ps < 4; ++ps) issue(ps);
#pragma unroll
        for (int ps = 0; ps < 10; ++ps) {
            if (ps <= 6) vmwait<24>(); else if (ps == 7) vmwait<16>(); else if (ps == 8) vmwait<8>(); else vmwait<0>();
            bar();
            if (ps + 4 < 10) issue(ps + 4);
#pragma unroll
            for (int tl = 0; tl < 4; ++tl) {
                const v8u f = tr_frag<512>(lds + (ps % 5) * 32768, 0, lane, 64 * wave + 32 * (tl >> 1) + 8 * (lane & 3) + 4 * (tl & 1));
                W[ps * 8 + tl] = lo4(f); W[ps * 8 + 4 + tl] = hi4(f);
                if (ps < 8) asm volatile("" : "+a"(W[ps * 8 + tl]), "+a"(W[ps * 8 + 4 + tl]));
            }
        }
        bar();
    }
    const unsigned flag = (j < 24 ? C_FK : C_FX) + jj * 16;
    const int b0 = 8 * jj + 2 * wave, b1 = b0 + 1;
    const unsigned ob0 = (j < 24 ? B_KX : B_VX) + (b0 / 6) * 12288 + (b0 % 6) * 1024, ob1 = (j < 24 ? B_KX : B_VX) + (b1 / 6) * 12288 + (b1 % 6) * 1024;
    v4u pf[10];
    unsigned long long hmask = 0;
    if (!fetch_hf(rs, 0, t, hmask, pf)) return;
    put_hf(lds, t, pf);
    bar();
    for (int s = 0; s < NSEG; ++s) {
        TS(128 + j, s, 0);
        if (s + 1 < NSEG) { if (!fetch_hf(rs, s + 1, t, hmask, pf)) return; }
        TS(128 + j, s, 1);
        v4f acc[4][2];
        {
            const char* AF = lds + (s & 1) * 40960;
            const char* const a0 = AF + (lane ^ (lane >> 4)) * 16;
            const char* const a1 = AF + (lane ^ (lane >> 4) ^ 4) * 16;
            v4u b[3][2];
#pragma unroll
            for (int kk = 0; kk < 2; ++kk) { b[kk][0] = lds16((kk & 1 ? a1 : a0) + kk * 1024); b[kk][1] = lds16((kk & 1 ? a1 : a0) + (20 + kk) * 1024); }
#pragma unroll
            for (int kk = 0; kk < 20; ++kk) {
                if (kk + 2 < 20) { b[(kk + 2) % 3][0] = lds16((kk & 1 ? a1 : a0) + (kk + 2) * 1024); b[(kk + 2) % 3][1] = lds16((kk & 1 ? a1 : a0) + (22 + kk) * 1024); }
                SB();
#pragma unroll
                for (int tl = 0; tl < 4; ++tl)
#pragma unroll
                    for (int rt = 0; rt < 2; ++rt) {
                        if (kk == 0) mf_av0(acc[tl][rt], W[tl], b[0][rt]);
                        else if (kk < 16) mf_av(acc[tl][rt], W[kk * 4 + tl], b[kk % 3][rt]);
                        else mf_vv(acc[tl][rt], W[kk * 4 + tl], b[kk % 3][rt]);
                    }
                SB();
            }
            settle2(acc[3][0], acc[3][1]); late3(acc[0][0], acc[1][0], acc[2][0]); late3(acc[0][1], acc[1][1], acc[2][1]);
        }
        TS(128 + j, s, 2);
        if (s + 1 < NSEG) put_hf(lds + ((s + 1) & 1) * 40960, t, pf);
        drain();
        if (s > 8 && lane == 0) st4(rs, flag + wave * 4, s);
#pragma unroll
        for (int bb = 0; bb < 2; ++bb)
#pragma unroll
            for (int rt = 0; rt < 2; ++rt) {
                const v4f x = acc[2 * bb][rt], y = acc[2 * bb + 1][rt];
                st16s(rs, lane * 16, (bb ? ob1 : ob0) + (unsigned)s * (NH * 12288) + rt * 6144, (v4u){pk2(x[0], x[1]), pk2(x[2], x[3]), pk2(y[0], y[1]), pk2(y[2], y[3])});
            }
        TS(128 + j, s, 3);
        if (s <= 8 || s + 1 == NSEG) post(rs, flag, wave, lane, s + 1);
        TS(128 + j, s, 4);
        bar();
    }
}

DEV void role_O(const Params& p, rsrc_t rs, int set, int gam, char* lds, int t) {
    const int lane = t & 63, wave = __builtin_amdgcn_readfirstlane(t >> 6), n = lane & 15, g = lane >> 4;
    const int c = gam * 4 + wave;
    unsigned got;
    if (!wait_word(rs, C_MODB, 40u, got)) return;
    const v4u gau = ld16(rs, B_MOD + (2 * D + 16 * c + 4 * g) * 4);
    unsigned seenU = 0, seenW = 0, seenP = 0;
    for (int s = set; s < NSEG; s += 2) {
        TS(256 + set * 10 + gam, s, 0);
        if (!wait_groups(rs, C_FU, 32, s + 1, lane, seenU)) return;
        v4u z[40];
#pragma unroll
        for (int i = 0; i < 32; ++i) z[i] = ld16s(rs, lane * 16, B_ZX + (unsigned)((s * 40 + c) * NH + i) * 1024u);
        v2u xr[2];
#pragma unroll
        for (int rt = 0; rt < 2; ++rt) xr[rt] = *(const v2u*)(p.x + (size_t)(32 * s + 16 * rt + n) * D + 16 * c + 4 * g);
        TS(256 + set * 10 + gam, s, 1);
        if (!wait_groups(rs, C_FW + c * 16, 1, s + 1, lane, seenW)) return;
        TS(256 + set * 10 + gam, s, 2);
#pragma unroll
        for (int i = 0; i < 8; ++i) z[32 + i] = ld16s(rs, lane * 16, B_ZM + (unsigned)(s * 40 + c) * 8192u + i * 1024);
        if (!wait_groups(rs, C_FP, 32, s + 1, lane, seenP)) return;
        TS(256 + set * 10 + gam, s, 3);
        {
            v4u pf[20];
#pragma unroll
            for (int k = 0; k < 20; ++k) pf[k] = k < 16 ? ld16s(rs, t * 16, B_PX + (unsigned)s * 65536u + k * 4096) : ld16s(rs, t * 16, B_PM + (unsigned)s * 16384u + (k - 16) * 4096);
#pragma unroll
            for (int k = 0; k < 20; ++k) sts16(lds + (k * 256 + t) * 16, pf[k]);
        }
        bar();
        v4f a0 = {0, 0, 0, 0}, a1 = a0;
#pragma unroll
        for (int i = 0; i < 32; ++i) {
            a0 = mfma32(z[i], lds16(lds + i * 1024 + lane * 16), a0);
            a1 = mfma32(z[i], lds16(lds + (32 + i) * 1024 + lane * 16), a1);
        }
#pragma unroll
        for (int i = 0; i < 8; ++i) {
            a0 = mfma32(z[32 + i], lds16(lds + 65536 + i * 1024 + lane * 16), a0);
            a1 = mfma32(z[32 + i], lds16(lds + 65536 + (8 + i) * 1024 + lane * 16), a1);
        }
#pragma unroll
        for (int rt = 0; rt < 2; ++rt) {
            const v4f a = rt ? a1 : a0;
            const float r0 = bflo(xr[rt].x) + rb(__uint_as_float(gau.x) * rb(a[0])), r1 = bfhi(xr[rt].x) + rb(__uint_as_float(gau.y) * rb(a[1]));
            const float r2 = bflo(xr[rt].y) + rb(__uint_as_float(gau.z) * rb(a[2])), r3 = bfhi(xr[rt].y) + rb(__uint_as_float(gau.w) * rb(a[3]));
            st8s(rs, n * 32 + g * 8, B_X1 + (unsigned)(s * 40 + c) * 1024u + rt * 512, (v2u){pk2(r0, r1), pk2(r2, r3)});
        }
        TS(256 + set * 10 + gam, s, 4);
        post(rs, C_FO + (set * 10 + gam) * 16, wave, lane, s + 1);
        TS(256 + set * 10 + gam, s, 5);
        bar();
    }
}

DEV void role_Q(const Params& p, rsrc_t rs, int h, char* lds, int t) {
    const int lane = t & 63, wave = __builtin_amdgcn_readfirstlane(t >> 6), n = lane & 15, g = lane >> 4;
    if (h >= 24) { __builtin_amdgcn_s_sleep(127); __builtin_amdgcn_s_sleep(127); }
    v4u W[60];
    load_proj_weights<HD>(p.Wq + h * DH, lds, lane, wave, W);
    char* const SPB = lds + 81920;
    const int bfull = (wave & 1) ? (3 * wave + 1) / 2 : (3 * wave) / 2, bhalf = (wave & 1) ? (3 * wave - 1) / 2 : (3 * wave) / 2 + 1;
    const int gh = 2 * (wave & 1) + (g >> 1);
    const float sc2 = 0.07216878364870322f * 1.4426950408889634f;
    v4u pf[10];
    unsigned seenK = 0;
    unsigned long long hmask = 0;
    if (!fetch_hf(rs, 0, t, hmask, pf)) return;
    put_hf(lds, t, pf);
    bar();
    for (int s = 0; s < NSEG; ++s) {
        TS(208 + h, s, 0);
        if (s + 1 < NSEG) { if (!fetch_hf(rs, s + 1, t, hmask, pf)) return; }
        if (!wait_head(rs, C_FK, h, s + 1, lane, seenK)) return;
        const unsigned kb = B_KX + (unsigned)(s * NH + h) * 12288u;
        v4u kf[3]; v2u kh[3];
#pragma unroll
        for (int kt = 0; kt < 2; ++kt) {
            kf[kt] = ld16s(rs, lane * 16, kb + (kt * 6 + bfull) * 1024);
            kh[kt] = ld8s(rs, (gh * 16 + n) * 16 + (g & 1) * 8, kb + (kt * 6 + bhalf) * 1024);
        }
        v4f acc[3][2];
        proj_mfma(lds + (s & 1) * 40960, lane, W, acc);
        if (s + 1 < NSEG) put_hf(lds + ((s + 1) & 1) * 40960, t, pf);
        unsigned got;
        TS(208 + h, s, 1);
        if (!wait_word(rs, C_FKM + h * 128, s + 1, got)) return;
        TS(208 + h, s, 2);
        const unsigned mb = B_KM + (unsigned)(s * NH + h) * 3072u;
        kf[2] = ld16s(rs, (g * 8 + (n & 7)) * 16, mb + bfull * 512);
        kh[2] = ld8s(rs, (gh * 8 + (n & 7)) * 16 + (g & 1) * 8, mb + bhalf * 512);
        char* const SP = SPB + (s & 1) * 24576;
#pragma unroll
        for (int kt = 0; kt < 3; ++kt)
#pragma unroll
            for (int rt = 0; rt < 2; ++rt) {
                const v4u qf = {pk2(acc[0][rt][0], acc[0][rt][1]), pk2(acc[0][rt][2], acc[0][rt][3]), pk2(acc[1][rt][0], acc[1][rt][1]), pk2(acc[1][rt][2], acc[1][rt][3])};
                const v2u qh = {pk2(acc[2][rt][0], acc[2][rt][1]), pk2(acc[2][rt][2], acc[2][rt][3])};
                const v4f z4 = {0, 0, 0, 0};
                *(LAS v4f*)(SP + ((wave * 6 + rt * 3 + kt) * 64 + lane) * 16) = mfma32(kf[kt], qf, z4) + mfma16(kh[kt], qh, z4);
            }
        bar();
        if (wave < 2) {
            const int rt = wave;
            v4f sv[3];
#pragma unroll
            for (int kt = 0; kt < 3; ++kt) {
                sv[kt] = (v4f){0, 0, 0, 0};
#pragma unroll
                for (int w = 0; w < 4; ++w) sv[kt] += *(const LAS v4f*)(SP + ((w * 6 + rt * 3 + kt) * 64 + lane) * 16);
            }
            const int tok = 16 * rt + n;
            const float NEG = -3.0e38f;
            float m = NEG;
#pragma unroll
            for (int i = 0; i < 4; ++i) {
                if (4 * g + i > tok) sv[0][i] = NEG;
                if (16 + 4 * g + i > tok) sv[1][i] = NEG;
                if (g >= 2) sv[2][i] = NEG;
                m = fmaxf(m, fmaxf(sv[0][i], fmaxf(sv[1][i], sv[2][i])));
            }
            m = xr16max(m); { float a = m, b = m; swap32(a, b); m = fmaxf(a, b); }
            float sum = 0;
#pragma unroll
            for (int kt = 0; kt < 3; ++kt)
#pragma unroll
                for (int i = 0; i < 4; ++i) { const float e = sv[kt][i] == NEG ? 0.f : __builtin_amdgcn_exp2f((sv[kt][i] - m) * sc2); sv[kt][i] = e; sum += e; }
            sum = xr16sum(sum); { float a = sum, b = sum; swap32(a, b); sum = a + b; }
            const float inv = 1.0f / sum;
            const v4u o = {pk2(sv[0][0] * inv, sv[0][1] * inv), pk2(sv[0][2] * inv, sv[0][3] * inv), pk2(sv[1][0] * inv, sv[1][1] * inv), pk2(sv[1][2] * inv, sv[1][3] * inv)};
            st16s(rs, lane * 16, B_PX + (unsigned)((s * 2 + rt) * NH + h) * 1024u, o);
            unsigned a0 = pk2(sv[2][0] * inv, sv[2][1] * inv), a1 = pk2(sv[2][2] * inv, sv[2][3] * inv), b0 = a0, b1 = a1;
            swap16(a0, b0); swap16(a1, b1);
            if (lane < 16) st16s(rs, n * 16, B_PM + (unsigned)((s * 2 + rt) * NH + h) * 256u, (v4u){a0, a1, b0, b1});
        }
        TS(208 + h, s, 3);
        post(rs, C_FP + h * 16, wave, lane, s + 1);
        TS(208 + h, s, 4);
    }
}

template <bool EXACT, class LD> DEV void ln_rows(LD ld, rsrc_t rs, unsigned shift_off, unsigned scale_off, char* AF, int lane, int wave, bool first, float (&sh)[40], float (&sc)[40]) {
    const int l16 = lane & 15, rq = lane >> 4;
    if (first) {
#pragma unroll
        for (int i = 0; i < 5; ++i)
#pragma unroll
            for (int q = 0; q < 2; ++q) {
                const v4u a = ld16(rs, shift_off + ((l16 + 16 * i) * 8 + q * 4) * 4), b = ld16(rs, scale_off + ((l16 + 16 * i) * 8 + q * 4) * 4);
#pragma unroll
                for (int j = 0; j < 4; ++j) { sh[i * 8 + q * 4 + j] = __uint_as_float(a[j]); sc[i * 8 + q * 4 + j] = rb(1.0f + __uint_as_float(b[j])); }
            }
    }
#pragma unroll
    for (int ps = 0; ps < 2; ++ps) {
        const int row = wave * 8 + ps * 4 + rq;
        v4u v[5];
#pragma unroll
        for (int i = 0; i < 5; ++i) v[i] = ld(row, l16 + 16 * i);
        float f[40]; float sum = 0;
#pragma unroll
        for (int i = 0; i < 5; ++i)
#pragma unroll
            for (int j = 0; j < 4; ++j) { f[i * 8 + 2 * j] = bflo(v[i][j]); f[i * 8 + 2 * j + 1] = bfhi(v[i][j]); sum += f[i * 8 + 2 * j] + f[i * 8 + 2 * j + 1]; }
        sum = sum16(sum);
        const float mean = sum * (1.0f / D);
        float q = 0;
#pragma unroll
        for (int i = 0; i < 40; ++i) { const float d = f[i] - mean; q += d * d; }
        q = sum16(q);
        const float mb = rb(mean), sd = rb(sqrtf(rb(rb(q * (1.0f / D)) + 1e-5f)));
        const float inv = 1.0f / sd;
#pragma unroll
        for (int i = 0; i < 5; ++i) {
            v4u o;
#pragma unroll
            for (int j = 0; j < 4; ++j) {
                const float a = EXACT ? rb(rb(rb(f[i * 8 + 2 * j] - mb) * inv) * sc[i * 8 + 2 * j]) + sh[i * 8 + 2 * j]
                                      : (f[i * 8 + 2 * j] - mb) * inv * sc[i * 8 + 2 * j] + sh[i * 8 + 2 * j];
                const float b = EXACT ? rb(rb(rb(f[i * 8 + 2 * j + 1] - mb) * inv) * sc[i * 8 + 2 * j + 1]) + sh[i * 8 + 2 * j + 1]
                                      : (f[i * 8 + 2 * j + 1] - mb) * inv * sc[i * 8 + 2 * j + 1] + sh[i * 8 + 2 * j + 1];
                o[j] = pk2(a, b);
            }
            const int ch = l16 + 16 * i;
            sts16(AF + (((row >> 4) * 20 + (ch >> 2)) * 64 + (((ch & 3) * 16 + (row & 15)) ^ (ch & 7))) * 16, o);
        }
    }
}

DEV bool prologue(const Params& p, rsrc_t rs, int pw, char* lds, int t) {
    const int lane = t & 63, wave = __builtin_amdgcn_readfirstlane(t >> 6);
    if (pw < 60) {
        const int cc = t & 7, kg = t >> 3;
        float acc[8] = {0, 0, 0, 0, 0, 0, 0, 0};
        v4u w[20];
#pragma unroll
        for (int k = 0; k < 20; ++k) w[k] = *(const v4u*)(p.wmod + (size_t)(kg * 20 + k) * (6 * D) + pw * 64 + cc * 8);
#pragma unroll
        for (int k = 0; k < 20; ++k) {
            const float xc = __uint_as_float((unsigned)p.c[kg * 20 + k] << 16);
            const float sl = rb(xc / (1.0f + __expf(-xc)));
#pragma unroll
            for (int j = 0; j < 4; ++j) { acc[2 * j] += sl * bflo(w[k][j]); acc[2 * j + 1] += sl * bfhi(w[k][j]); }
        }
        LAS float* red = (LAS float*)lds;
#pragma unroll
        for (int j = 0; j < 8; ++j) red[kg * 65 + cc * 8 + j] = acc[j];
        bar();
        if (t < 64) {
            float a = 0;
#pragma unroll
            for (int k = 0; k < 32; ++k) a += red[k * 65 + t];
            st4(rs, B_MOD + (pw * 64 + t) * 4, __float_as_uint(rb(a)));
            drain();
            if (t == 0) __hip_atomic_fetch_add((GAS unsigned*)(p.ws + (pw < 20 ? C_MODA : C_MODB)), 1u, __ATOMIC_RELAXED, __HIP_MEMORY_SCOPE_AGENT);
        }
        bar();
    }
    if (pw == 63) {
        if (lane == 0) {
            unsigned best[4] = {0xffffffffu, 0xffffffffu, 0xffffffffu, 0xffffffffu};
            for (int r = 0; r < 7; ++r)
#pragma unroll
                for (int cd = 0; cd < 4; ++cd) {
                    GAS unsigned* a = (GAS unsigned*)(p.ws + ((cd >> 1) ? PAGE + rmap(L_PROBE1, cd & 1) : rmap(L_PROBE0, cd & 1)) + ((r + wave) & 3) * 2048 + wave * 128);
                    unsigned z = 0, v; unsigned long long t0, t1;
                    asm volatile("s_memtime %1\n\ts_waitcnt lgkmcnt(0)\n\tglobal_atomic_add %0, %3, %4, off sc0 sc1\n\ts_waitcnt vmcnt(0)\n\ts_memtime %2\n\ts_waitcnt lgkmcnt(0)"
                                 : "=&v"(v), "=&s"(t0), "=&s"(t1) : "v"(a), "v"(z) : "memory");
                    best[cd] = min(best[cd], (unsigned)(t1 - t0));
                }
            *(LAS v4u*)(lds + wave * 16) = (v4u){best[0], best[1], best[2], best[3]};
        }
        bar();
        if (t == 0) {
            const LAS unsigned* qq = (const LAS unsigned*)lds;
            unsigned q[4];
#pragma unroll
            for (int cd = 0; cd < 4; ++cd) q[cd] = min(min(qq[cd], qq[4 + cd]), min(qq[8 + cd], qq[12 + cd]));
            st4(rs, C_PAR, 1u + (q[1] < q[0] ? 1u : 0u) + (q[3] < q[2] ? 2u : 0u));
        }
        bar();
    }
    const int s = (pw + 4) & 63;
    unsigned got;
    if (!wait_word(rs, C_MODA, 20u, got)) return false;
    float sh[40], sc[40];
    const uint16_t* xs = p.x + (size_t)s * 32 * D;
    ln_rows<true>([&](int row, int ch) { return *(const v4u*)(xs + row * D + ch * 8); }, rs, B_MOD, B_MOD + D * 4, lds, lane, wave, true, sh, sc);
    bar();
#pragma unroll
    for (int k = 0; k < 10; ++k) st16s(rs, t * 16, B_HF + (unsigned)s * 40960u + k * 4096, lds16(lds + (k * 256 + t) * 16));
    drain();
    bar();
    if (t == 0) st4(rs, C_FH + s * 4, 1u);
    return true;
}

DEV void role_L(const Params& p, rsrc_t rs, int set, char* lds, int t) {
    const int lane = t & 63, wave = __builtin_amdgcn_readfirstlane(t >> 6);
    unsigned got;
    if (!wait_word(rs, C_MODB, 40u, got)) return;
    float sh[40], sc[40];
    unsigned seenO = 0;
    for (int s = set; s < NSEG; s += 2) {
        TS(300 + set, s, 0);
        if (!wait_groups(rs, C_FO + set * 160, 10, s + 1, lane, seenO)) return;
        TS(300 + set, s, 1);
        const unsigned xb = B_X1 + (unsigned)s * 40960u;
        ln_rows<false>([&](int row, int ch) { return ld16s(rs, (ch >> 1) * 1024 + row * 32 + (ch & 1) * 16, xb); }, rs, B_MOD + 3 * D * 4, B_MOD + 4 * D * 4, lds, lane, wave, s == set, sh, sc);
        bar();
        TS(300 + set, s, 2);
#pragma unroll
        for (int k = 0; k < 10; ++k) st16s(rs, t * 16, B_H2 + (unsigned)s * 40960u + k * 4096, lds16(lds + (k * 256 + t) * 16));
        post(rs, C_FL + set * 16, wave, lane, s + 1);
        TS(300 + set, s, 3);
        bar();
    }
}

DEV void role_M1(const Params& p, rsrc_t rs, int phi, char* lds, int t) {
    const int lane = t & 63, wave = __builtin_amdgcn_readfirstlane(t >> 6), g = lane >> 4;
    for (int i = 0; i < 3; ++i) __builtin_amdgcn_s_sleep(127);
    v4u W[60];
    load_proj_weights<FF>(p.w1 + phi * 192, lds, lane, wave, W);
    const int efull = (wave & 1) ? 48 * wave + 16 : 48 * wave, ehalf = (wave & 1) ? 48 * wave : 48 * wave + 32;
    float bias[3][4];
#pragma unroll
    for (int i = 0; i < 4; ++i) {
        bias[0][i] = __uint_as_float((unsigned)p.b1[phi * 192 + efull + 8 * g + i] << 16);
        bias[1][i] = __uint_as_float((unsigned)p.b1[phi * 192 + efull + 8 * g + 4 + i] << 16);
        bias[2][i] = __uint_as_float((unsigned)p.b1[phi * 192 + ehalf + 4 * g + i] << 16);
    }
    unsigned seenL0 = 0, seenL1 = 0;
    for (int s = 0; s < NSEG; ++s) {
        TS(280 + phi, s, 0);
        unsigned sn = (s & 1) ? seenL1 : seenL0;
        if (!wait_groups(rs, C_FL + (s & 1) * 16, 1, s + 1, lane, sn)) return;
        if (s & 1) seenL1 = sn; else seenL0 = sn;
        TS(280 + phi, s, 1);
        char* AF = lds + (s & 1) * 40960;
        {
            v4u pf[10];
#pragma unroll
            for (int k = 0; k < 10; ++k) pf[k] = ld16s(rs, t * 16, B_H2 + (unsigned)s * 40960u + k * 4096);
            put_hf(AF, t, pf);
        }
        bar();
        TS(280 + phi, s, 2);
        v4f acc[3][2];
        proj_mfma(AF, lane, W, acc);
        TS(280 + phi, s, 3);
#pragma unroll
        for (int tl = 0; tl < 3; ++tl)
#pragma unroll
            for (int rt = 0; rt < 2; ++rt)
#pragma unroll
                for (int i = 0; i < 4; ++i) { float v = rb(rb(acc[tl][rt][i]) + bias[tl][i]); v = v > 0.f ? v : 0.f; acc[tl][rt][i] = v * v; }
        proj_store(rs, B_HID + (unsigned)(s * 8 + phi) * 12288u, lane, wave, acc);
        post(rs, C_FM1 + phi * 16, wave, lane, s + 1);
        TS(280 + phi, s, 4);
    }
}

DEV void role_M2(const Params& p, rsrc_t rs, int gam, char* lds, int t) {
    const int lane = t & 63, wave = __builtin_amdgcn_readfirstlane(t >> 6), n = lane & 15, g = lane >> 4;
    for (int i = 0; i < 3; ++i) __builtin_amdgcn_s_sleep(127);
    v4u W[48];
    {
        unsigned vo[6];
#pragma unroll
        for (int k = 0; k < 6; ++k) { const int i = (wave * 6 + k) * 64 + lane; vo[k] = (unsigned)((i >> 3) * D * 2 + stg_src<128>(i >> 3, i & 7) * 16); }
        const unsigned ldsb = (unsigned)(size_t)(LAS char*)lds + wave * 6144;
        auto issue = [&](int ps) {
            const uint16_t* src = p.w2 + (size_t)ps * 192 * D + gam * 64;
#pragma unroll
            for (int k = 0; k < 6; ++k) dma16(src, vo[k], ldsb + (ps % 5) * STG + k * 1024);
        };
#pragma unroll
        for (int ps = 0; ps < 4; ++ps) issue(ps);
#pragma unroll
        for (int ps = 0; ps < 8; ++ps) {
            if (ps <= 4) vmwait<18>(); else if (ps == 5) vmwait<12>(); else if (ps == 6) vmwait<6>(); else vmwait<0>();
            bar();
            if (ps + 4 < 8) issue(ps + 4);
            const char* tile = lds + (ps % 5) * STG;
#pragma unroll
            for (int j = 0; j < 3; ++j) {
                const v8u f = tr_frag<128>(tile, 64 * j, lane, 16 * wave + 4 * (lane & 3));
                W[(ps * 3 + j) * 2] = lo4(f); W[(ps * 3 + j) * 2 + 1] = hi4(f);
                asm volatile("" : "+a"(W[(ps * 3 + j) * 2]), "+a"(W[(ps * 3 + j) * 2 + 1]));
            }
        }
        bar();
    }
    const int col = gam * 64 + wave * 16 + 4 * g;
    const v2u b2u = *(const v2u*)(p.b2 + col);
    unsigned got;
    if (!wait_word(rs, C_MODB, 40u, got)) return;
    const v4u gbu = ld16(rs, B_MOD + (5 * D + col) * 4);
    unsigned seenM = 0;
    for (int s = 0; s < NSEG; ++s) {
        TS(290 + gam, s, 0);
        if (!wait_groups(rs, C_FM1, 8, s + 1, lane, seenM)) return;
        TS(290 + gam, s, 1);
        v2u xr[2];
#pragma unroll
        for (int rt = 0; rt < 2; ++rt) xr[rt] = ld8s(rs, (16 * rt + n) * 32 + g * 8, B_X1 + (unsigned)(s * 40 + gam * 4 + wave) * 1024u);
#pragma unroll
        for (int half = 0; half < 2; ++half) {
            v4u pf[12];
#pragma unroll
            for (int k = 0; k < 12; ++k) pf[k] = ld16s(rs, t * 16, B_HID + (unsigned)s * 98304u + (half * 12 + k) * 4096);
#pragma unroll
            for (int k = 0; k < 12; ++k) sts16(lds + ((half * 12 + k) * 256 + t) * 16, pf[k]);
        }
        bar();
        TS(290 + gam, s, 2);
        v4f a0, a1;
        v4u b[6][2];
        auto fb = [&](int kk) { return lds + ((kk / 6) * 12 + (kk % 6)) * 1024 + lane * 16; };
#pragma unroll
        for (int kk = 0; kk < 5; ++kk) { b[kk][0] = lds16(fb(kk)); b[kk][1] = lds16(fb(kk) + 6144); }
#pragma unroll
        for (int kk = 0; kk < 48; ++kk) {
            if (kk + 5 < 48) { b[(kk + 5) % 6][0] = lds16(fb(kk + 5)); b[(kk + 5) % 6][1] = lds16(fb(kk + 5) + 6144); }
            SB();
            if (kk == 0) { mf_av0(a0, W[0], b[0][0]); mf_av0(a1, W[0], b[0][1]); }
            else { mf_av(a0, W[kk], b[kk % 6][0]); mf_av(a1, W[kk], b[kk % 6][1]); }
            SB();
        }
        settle2(a0, a1);
#pragma unroll
        for (int rt = 0; rt < 2; ++rt) {
            const v4f a = rt ? a1 : a0;
            const float r0 = bflo(xr[rt].x) + rb(__uint_as_float(gbu.x) * rb(rb(a[0]) + bflo(b2u.x))), r1 = bfhi(xr[rt].x) + rb(__uint_as_float(gbu.y) * rb(rb(a[1]) + bfhi(b2u.x)));
            const float r2 = bflo(xr[rt].y) + rb(__uint_as_float(gbu.z) * rb(rb(a[2]) + bflo(b2u.y))), r3 = bfhi(xr[rt].y) + rb(__uint_as_float(gbu.w) * rb(rb(a[3]) + bfhi(b2u.y)));
            *(v2u*)(p.out + (size_t)(32 * s + 16 * rt + n) * D + col) = (v2u){pk2(r0, r1), pk2(r2, r3)};
        }
        TS(290 + gam, s, 3);
        bar();
    }
}

__global__ void __launch_bounds__(256, 1) fused_kernel(Params p) {
    extern __shared__ __attribute__((aligned(16))) char lds[];
    const int t = threadIdx.x;
    const unsigned long long litek_ = 0;
    const rsrc_t rs = __builtin_amdgcn_make_buffer_rsrc(p.ws, 0, B_END, 0x00020000);
    if (t == 0) {
        const unsigned xcc = (unsigned)__builtin_amdgcn_s_getreg(20 | (3 << 11)) & 0xfu;
        const unsigned tk = __hip_atomic_fetch_add((GAS unsigned*)(p.ws + (xcc < 4 ? C_TICKA : C_TICKB)), 1u, __ATOMIC_RELAXED, __HIP_MEMORY_SCOPE_AGENT);
        *(LAS unsigned*)lds = xcc < 4 ? tk : 255u - tk;
    }
    bar();
    int role = __builtin_amdgcn_readfirstlane((int)*(LAS unsigned*)lds) & 255;
    {
        const int q = (role >> 4) & 7, r = role & 15;
        if (role < 128) role = r < 4 ? q * 4 + r : r < 8 ? 32 + q * 4 + (r - 4) : r < 13 ? 64 + q * 5 + (r - 8) : 104 + q * 3 + (r - 13);
        else role = r < 1 ? 128 + q : r < 7 ? 136 + q * 6 + (r - 1) : r < 11 ? 184 + q * 4 + (r - 7) : 216 + q * 5 + (r - 11);
    }
    bar();
    TS(role, 65, 0);
    if (role >= 64 && role < 128) {
        if (!prologue(p, rs, role - 64, lds, t)) return;
        bar();
    }
    if (role < 32) role_S(p, rs, role, lds, t);
    else if (role < 64) role_V(p, rs, role - 32, lds, t);
    else if (role < 104) role_R(p, rs, role - 64, lds, t, litek_);
    else if (role < 136) role_Q(p, rs, role - 104, lds, t);
    else if (role < 184) role_KX(p, rs, role - 136, lds, t);
    else if (role < 216) role_U(p, rs, role - 184, lds, t);
    else if (role < 236) role_O(p, rs, (role - 216) & 1, (role - 216) >> 1, lds, t);
    else if (role < 244) role_M1(p, rs, role - 236, lds, t);
    else if (role < 254) role_M2(p, rs, role - 244, lds, t);
    else role_L(p, rs, role - 254, lds, t);
}

extern "C" void kernel_launch(void* const* d_in, const int* in_sizes, int n_in, void* d_out, int out_size, void* d_ws, size_t ws_size, hipStream_t stream) {
    Params p;
    p.x = (const uint16_t*)d_in[0]; p.mem0 = (const uint16_t*)d_in[1];
    p.Wq = (const uint16_t*)d_in[2]; p.Wk = (const uint16_t*)d_in[3]; p.Wv = (const uint16_t*)d_in[4]; p.Wo = (const uint16_t*)d_in[5];
    p.w1 = (const uint16_t*)d_in[8]; p.b1 = (const uint16_t*)d_in[9]; p.w2 = (const uint16_t*)d_in[10]; p.b2 = (const uint16_t*)d_in[11];
    p.c = (const uint16_t*)d_in[12]; p.wmod = (const uint16_t*)d_in[13];
    p.out = (uint16_t*)d_out;
    p.ws = (unsigned char*)(((uintptr_t)d_ws + PAGE - 1) & ~(uintptr_t)(PAGE - 1));
    (void)hipFuncSetAttribute((const void*)fused_kernel, hipFuncAttributeMaxDynamicSharedMemorySize, LDS_BYTES);
    (void)hipMemsetAsync(p.ws, 0, ZERO_BYTES, stream);
#define SOLO_GRID 256
    hipLaunchKernelGGL(fused_kernel, dim3(SOLO_GRID), dim3(256), LDS_BYTES, stream, p);
}
```

```cpp
#include <hip/hip_runtime.h>
#include <hip/hip_bf16.h>
#include <stdint.h>

#define LAS __attribute__((address_space(3)))
#define GAS __attribute__((address_space(1)))
#define DEV __device__ __forceinline__
typedef unsigned v4u __attribute__((ext_vector_type(4)));
typedef unsigned v2u __attribute__((ext_vector_type(2)));
typedef unsigned v8u __attribute__((ext_vector_type(8)));
typedef float v4f __attribute__((ext_vector_type(4)));
typedef short v4s __attribute__((ext_vector_type(4)));
typedef __bf16 bf8 __attribute__((ext_vector_type(8)));
typedef __bf16 bf16v __attribute__((ext_vector_type(16)));
typedef __bf16 bf2 __attribute__((ext_vector_type(2)));
typedef __amdgpu_buffer_rsrc_t rsrc_t;

constexpr int D = 640, NH = 32, DH = 192, HD = NH * DH, NSEG = 64, FF = 1536;

constexpr unsigned PAGE = 2u << 20;
constexpr unsigned C_ABORT = 512, C_PAR = 768, C_MODA = 1024, C_MODB = 1280, C_TICK = 9216;
constexpr unsigned C_FH = 2048;
constexpr unsigned C_FK = 4096, C_FX = 4608, C_FU = 5120, C_FKM = 12288, C_FP = 6144, C_FM1 = 6656, C_FO = 7168, C_FW = 7680, C_FL = 8448;
constexpr unsigned L_MEM = 8192, MEM_SLOT = 16384;
constexpr unsigned L_P = L_MEM + 4 * MEM_SLOT, P_SLOT = 32768;
constexpr unsigned L_WM = L_P + 4 * P_SLOT, WM_SLOT = 327680;
constexpr unsigned L_PROBE0 = L_WM + 2 * WM_SLOT, L_PROBE1 = 2 * WM_SLOT;
constexpr unsigned ZERO_BYTES = PAGE + 2 * (L_PROBE1 + 8192);
constexpr unsigned B_MOD = 2 * PAGE;
constexpr unsigned B_HF = B_MOD + 16384;
constexpr unsigned B_KX = B_HF + NSEG * 40960;
constexpr unsigned B_VX = B_KX + NSEG * NH * 12288;
constexpr unsigned B_KM = B_VX + NSEG * NH * 12288;
constexpr unsigned B_ZX = B_KM + NSEG * NH * 3072;
constexpr unsigned B_ZM = B_ZX + NSEG * 40 * NH * 1024;
constexpr unsigned B_PX = B_ZM + NSEG * 40 * 8192;
constexpr unsigned B_PM = B_PX + NSEG * NH * 2048;
constexpr unsigned B_X1 = B_PM + NSEG * 16384;
constexpr unsigned B_HID = B_X1 + 2048 * D * 2;
constexpr unsigned B_H2 = B_HID + NSEG * 8 * 12288;
constexpr unsigned B_DBG = B_H2 + NSEG * 40960;
constexpr unsigned B_TS = B_DBG + 65 * 10240;
constexpr unsigned B_END = B_TS + 320 * 66 * 8 * 8;

constexpr unsigned OOB = 0x80000000u;
constexpr int LDS_BYTES = 160 * 1024;
constexpr unsigned SPIN_CAP = 1u << 18;
#define PIPE 0
#define RS 0
#define PS 0
#define ES 0
#define POLLSLEEP 8

struct Params {
    const uint16_t* x; const uint16_t* mem0; const uint16_t* Wq; const uint16_t* Wk; const uint16_t* Wv; const uint16_t* Wo;
    const uint16_t* w1; const uint16_t* b1; const uint16_t* w2; const uint16_t* b2; const uint16_t* c; const uint16_t* wmod;
    uint16_t* out; unsigned char* ws;
};

template <int V> struct IC { static constexpr int value = V; };
template <int I, int N, class F> DEV void sfor(F&& f) { if constexpr (I < N) { f(IC<I>{}); sfor<I + 1, N>(f); } }

DEV v4u ld16(rsrc_t r, unsigned off) { return __builtin_amdgcn_raw_buffer_load_b128(r, off, 0, 16); }
DEV v2u ld8(rsrc_t r, unsigned off) { return __builtin_amdgcn_raw_buffer_load_b64(r, off, 0, 16); }
DEV unsigned ld4(rsrc_t r, unsigned off) { return __builtin_amdgcn_raw_buffer_load_b32(r, off, 0, 16); }
DEV void st16(rsrc_t r, unsigned off, v4u v) { __builtin_amdgcn_raw_buffer_store_b128(v, r, off, 0, 16); }
DEV void st4(rsrc_t r, unsigned off, unsigned v) { __builtin_amdgcn_raw_buffer_store_b32(v, r, off, 0, 16); }
DEV v4u ld16s(rsrc_t r, unsigned v, unsigned so) { return __builtin_amdgcn_raw_buffer_load_b128(r, v, so, 16); }
DEV v2u ld8s(rsrc_t r, unsigned v, unsigned so) { return __builtin_amdgcn_raw_buffer_load_b64(r, v, so, 16); }
DEV unsigned ld4s(rsrc_t r, unsigned v, unsigned so) { return __builtin_amdgcn_raw_buffer_load_b32(r, v, so, 16); }
DEV void st16s(rsrc_t r, unsigned v, unsigned so, v4u x) { __builtin_amdgcn_raw_buffer_store_b128(x, r, v, so, 16); }
DEV void st8s(rsrc_t r, unsigned v, unsigned so, v2u x) { __builtin_amdgcn_raw_buffer_store_b64(x, r, v, so, 16); }
DEV void st4s(rsrc_t r, unsigned v, unsigned so, unsigned x) { __builtin_amdgcn_raw_buffer_store_b32(x, r, v, so, 16); }
DEV void drain() { asm volatile("s_waitcnt vmcnt(0)" ::: "memory"); }
DEV void bar() { asm volatile("s_waitcnt lgkmcnt(0)" ::: "memory"); __builtin_amdgcn_s_barrier(); asm volatile("" ::: "memory"); }
DEV float bflo(unsigned u) { return __uint_as_float(u << 16); }
DEV float bfhi(unsigned u) { return __uint_as_float(u & 0xffff0000u); }
DEV unsigned pk2(float a, float b) { bf2 v; v[0] = (__bf16)a; v[1] = (__bf16)b; return __builtin_bit_cast(unsigned, v); }
DEV float rb(float a) { return (float)(__bf16)a; }
DEV unsigned short bf1(float a) { return __builtin_bit_cast(unsigned short, (__bf16)a); }
DEV unsigned nz(unsigned u) { return max(u, 1u); }
DEV v4u nz4(v4u v) { v.x = nz(v.x); v.y = nz(v.y); v.z = nz(v.z); v.w = nz(v.w); return v; }
DEV unsigned min4(v4u v) { return min(min(v.x, v.y), min(v.z, v.w)); }
DEV unsigned rmap(unsigned L, unsigned par) { return ((L >> 13) << 14) + (L & 8191u) + par * 8192u; }
DEV v4f mfma32(v4u a, v4u b, v4f c) { return __builtin_amdgcn_mfma_f32_16x16x32_bf16(__builtin_bit_cast(bf8, a), __builtin_bit_cast(bf8, b), c, 0, 0, 0); }
DEV v4f mfma16(v2u a, v2u b, v4f c) { return __builtin_amdgcn_mfma_f32_16x16x16bf16_1k(__builtin_bit_cast(v4s, a), __builtin_bit_cast(v4s, b), c, 0, 0, 0); }
DEV v4f smf(v4u a, v8u b, v4f c, int idx) { return __builtin_amdgcn_smfmac_f32_16x16x64_bf16(__builtin_bit_cast(bf8, a), __builtin_bit_cast(bf16v, b), c, idx, 0, 0); }
#define SB() __builtin_amdgcn_sched_barrier(0)
DEV void smf_a(v4f& c, v4u a, v8u w, int idx) { asm volatile("v_smfmac_f32_16x16x64_bf16 %0, %1, %2, %3" : "+v"(c) : "v"(a), "a"(w), "v"(idx)); }
DEV void smf_v(v4f& c, v4u a, v8u w, int idx) { asm volatile("v_smfmac_f32_16x16x64_bf16 %0, %1, %2, %3" : "+v"(c) : "v"(a), "v"(w), "v"(idx)); }
DEV void smf_a0(v4f& c, v4u a, v8u w, int idx) { c = (v4f){0, 0, 0, 0}; asm volatile("s_nop 1\n\tv_smfmac_f32_16x16x64_bf16 %0, %1, %2, %3" : "+v"(c) : "v"(a), "a"(w), "v"(idx)); }
DEV void smf_v0(v4f& c, v4u a, v8u w, int idx) { c = (v4f){0, 0, 0, 0}; asm volatile("s_nop 1\n\tv_smfmac_f32_16x16x64_bf16 %0, %1, %2, %3" : "+v"(c) : "v"(a), "v"(w), "v"(idx)); }
DEV void mf_av0(v4f& c, v4u a, v4u b) { asm volatile("v_mfma_f32_16x16x32_bf16 %0, %1, %2, 0" : "=&v"(c) : "a"(a), "v"(b)); }
DEV void mf_vv0(v4f& c, v4u a, v4u b) { asm volatile("v_mfma_f32_16x16x32_bf16 %0, %1, %2, 0" : "=&v"(c) : "v"(a), "v"(b)); }
DEV void mf_av(v4f& c, v4u a, v4u b) { asm volatile("v_mfma_f32_16x16x32_bf16 %0, %1, %2, %0" : "+v"(c) : "a"(a), "v"(b)); }
DEV void mf_va0(v4f& c, v4u a, v4u b) { asm volatile("v_mfma_f32_16x16x32_bf16 %0, %1, %2, 0" : "=&v"(c) : "v"(a), "a"(b)); }
DEV void mf_va(v4f& c, v4u a, v4u b) { asm volatile("v_mfma_f32_16x16x32_bf16 %0, %1, %2, %0" : "+v"(c) : "v"(a), "a"(b)); }
DEV void mf_vv(v4f& c, v4u a, v4u b) { asm volatile("v_mfma_f32_16x16x32_bf16 %0, %1, %2, %0" : "+v"(c) : "v"(a), "v"(b)); }
DEV void mf_vvn(v4f& c, v4u a, v4u b) { asm volatile("s_nop 1\n\tv_mfma_f32_16x16x32_bf16 %0, %1, %2, %0" : "+v"(c) : "v"(a), "v"(b)); }
DEV void settle2(v4f& a, v4f& b) { asm volatile("s_nop 7" : "+v"(a), "+v"(b)); }
DEV void settle3(v4f& a, v4f& b, v4f& c) { asm volatile("s_nop 7" : "+v"(a), "+v"(b), "+v"(c)); }
DEV void late2(v4f& a, v4f& b) { asm volatile("" : "+v"(a), "+v"(b)); }
template <int CTRL> DEV float dpp(float v) { return __uint_as_float(__builtin_amdgcn_update_dpp(0u, __float_as_uint(v), CTRL, 0xf, 0xf, true)); }
#define DPPSTEP(op, ctl) "s_nop 1\n\t" op " %0, %0, %0 " ctl " row_mask:0xf bank_mask:0xf\n\t"
DEV float max8(float m) {
    asm volatile(DPPSTEP("v_max_f32_dpp", "quad_perm:[1,0,3,2]") DPPSTEP("v_max_f32_dpp", "quad_perm:[2,3,0,1]") DPPSTEP("v_max_f32_dpp", "row_half_mirror") : "+v"(m));
    return m;
}
DEV float sum8(float m) {
    asm volatile(DPPSTEP("v_add_f32_dpp", "quad_perm:[1,0,3,2]") DPPSTEP("v_add_f32_dpp", "quad_perm:[2,3,0,1]") DPPSTEP("v_add_f32_dpp", "row_half_mirror") : "+v"(m));
    return m;
}
DEV float max16(float m) {
    asm volatile(DPPSTEP("v_max_f32_dpp", "quad_perm:[1,0,3,2]") DPPSTEP("v_max_f32_dpp", "quad_perm:[2,3,0,1]")
                 DPPSTEP("v_max_f32_dpp", "row_half_mirror") DPPSTEP("v_max_f32_dpp", "row_mirror") : "+v"(m));
    return m;
}
DEV float sum16(float m) {
    asm volatile(DPPSTEP("v_add_f32_dpp", "quad_perm:[1,0,3,2]") DPPSTEP("v_add_f32_dpp", "quad_perm:[2,3,0,1]")
                 DPPSTEP("v_add_f32_dpp", "row_half_mirror") DPPSTEP("v_add_f32_dpp", "row_mirror") : "+v"(m));
    return m;
}
DEV void mf16_vv(v4f& c, v2u a, v2u b) { asm volatile("v_mfma_f32_16x16x16_bf16 %0, %1, %2, %0" : "+v"(c) : "v"(a), "v"(b)); }
DEV void late3(v4f& a, v4f& b, v4f& c) { asm volatile("" : "+v"(a), "+v"(b), "+v"(c)); }
DEV void late1(v4f& a) { asm volatile("" : "+v"(a)); }
DEV void settle1(v4f& a) { asm volatile("s_nop 7" : "+v"(a)); }
DEV void lgkm0() { asm volatile("s_waitcnt lgkmcnt(0)" ::: "memory"); }
DEV v4u cat4(v2u a, v2u b) { return __builtin_shufflevector(a, b, 0, 1, 2, 3); }
DEV void st8(rsrc_t r, unsigned off, v2u v) { __builtin_amdgcn_raw_buffer_store_b64(v, r, off, 0, 16); }
DEV v8u cat8(v4u a, v4u b) { return __builtin_shufflevector(a, b, 0, 1, 2, 3, 4, 5, 6, 7); }
DEV v4u lo4(v8u v) { return __builtin_shufflevector(v, v, 0, 1, 2, 3); }
DEV v4u hi4(v8u v) { return __builtin_shufflevector(v, v, 4, 5, 6, 7); }
DEV v4u lds16(const char* p) { return *(const LAS v4u*)(p); }
DEV void sts16(char* p, v4u v) { *(LAS v4u*)(p) = v; }
DEV v2u trread(const char* p) { return __builtin_bit_cast(v2u, __builtin_amdgcn_ds_read_tr16_b64_v4i16((LAS v4s*)(p))); }
DEV void swap32(float& a, float& b) { auto r = __builtin_amdgcn_permlane32_swap(__float_as_uint(a), __float_as_uint(b), false, false); a = __uint_as_float(r[0]); b = __uint_as_float(r[1]); }
DEV void swap16(unsigned& a, unsigned& b) { auto r = __builtin_amdgcn_permlane16_swap(a, b, false, false); a = r[0]; b = r[1]; }

DEV float xr16max(float v) { unsigned a = __float_as_uint(v), b = a; swap16(a, b); return fmaxf(__uint_as_float(a), __uint_as_float(b)); }
DEV float xr16sum(float v) { unsigned a = __float_as_uint(v), b = a; swap16(a, b); return __uint_as_float(a) + __uint_as_float(b); }
#define TS(role, s, k) do {} while (0)
#define TSLANE 0
#define CYDECL
#define CY(k) do {} while (0)
#define CYOUT(role) do {} while (0)
#define WAITOK(x) (x)
DEV bool spin_fail(rsrc_t rs, unsigned& spins) {
    ++spins;
    if (spins > SPIN_CAP) { st4(rs, C_ABORT, 1u); return true; }
    if ((spins & 127u) == 0u) { if (__builtin_amdgcn_readfirstlane(ld4(rs, C_ABORT))) return true; }
    return false;
}
DEV bool wait_groups(rsrc_t rs, unsigned base, int n, unsigned need, int lane, unsigned& seen) {
    if (seen >= need) return true;
    const unsigned off = base + (unsigned)(lane < n ? lane : 0) * 16u;
    for (unsigned spins = 0;;) {
        unsigned m = min4(ld16(rs, off));
        asm volatile("" ::: "memory");
        if (WAITOK(__all(m >= need))) {
            unsigned lo = need;
#pragma unroll
            for (int k = 4; k >= 1; k >>= 1) if (__all(m >= lo + k)) lo += k;
            seen = lo;
            return true;
        }
        __builtin_amdgcn_s_sleep(POLLSLEEP);
        if (spin_fail(rs, spins)) return false;
    }
}
DEV bool wait_word(rsrc_t rs, unsigned off, unsigned need, unsigned& got) {
    for (unsigned spins = 0;;) {
        got = __builtin_amdgcn_readfirstlane(ld4(rs, off));
        asm volatile("" ::: "memory");
        if (WAITOK(got >= need)) return true;
        __builtin_amdgcn_s_sleep(POLLSLEEP);
        if (spin_fail(rs, spins)) return false;
    }
}
DEV bool wait_par(rsrc_t rs, int lane, unsigned& par) {
    for (unsigned spins = 0;;) {
        const v4u v = ld16(rs, C_PAR);
        asm volatile("" ::: "memory");
        if (WAITOK(__all(min4(v) != 0u))) { par = __builtin_amdgcn_readfirstlane((v.y < v.x ? 1u : 0u) + (v.w < v.z ? 2u : 0u)); return true; }
        __builtin_amdgcn_s_sleep(POLLSLEEP);
        if (spin_fail(rs, spins)) return false;
    }
}
DEV bool wait_head(rsrc_t rs, unsigned base, int h, unsigned need, int lane, unsigned& seen) {
    const int j0 = (6 * h) >> 3, j1 = (6 * h + 5) >> 3;
    return wait_groups(rs, base + j0 * 16, j1 - j0 + 1, need, lane, seen);
}
DEV void post(rsrc_t rs, unsigned group, int wave, int lane, unsigned val) {
    drain();
    if (lane == 0) st4(rs, group + wave * 4, val);
}

#define DMAPOL ""
DEV void dma16(const void* gbase, unsigned voff, unsigned ldsaddr) {
    unsigned keep;
    asm volatile("s_mov_b32 %0, m0\n\ts_mov_b32 m0, %3\n\ts_nop 0\n\tglobal_load_lds_dwordx4 %1, %2" DMAPOL "\n\ts_mov_b32 m0, %0"
                 : "=&s"(keep) : "v"(voff), "s"(gbase), "s"(ldsaddr) : "memory");
}
DEV void vmwait_n(int n) { asm volatile("s_waitcnt vmcnt(%0)" :: "n"(n) : "memory"); }
template <int N> DEV void vmwait() { asm volatile("s_waitcnt vmcnt(%0)" :: "n"(N) : "memory"); }
template <int ROWB> DEV int stg_f(int row) { return (ROWB == 256 || ROWB == 512) ? ((row & 3) | (((row >> 3) & 1) << 2)) : (((row >> 1) & 1) | (((row >> 3) & 1) << 1)); }
template <int ROWB> DEV int stg_src(int row, int ch) { return ch ^ (stg_f<ROWB>(row) << 1); }
template <int ROWB> DEV v8u tr_frag(const char* tile, int r0, int lane, int colrun) {
    const int g = lane >> 4, q = (lane & 15) >> 2, row = 8 * g + q;
    const char* a = tile + (r0 + row) * ROWB + ((colrun * 2) ^ (stg_f<ROWB>(row) << 5));
    const v2u x0 = trread(a), x1 = trread(a + 4 * ROWB), x2 = trread(a + 32 * ROWB), x3 = trread(a + 36 * ROWB);
    v8u r; r[0] = x0.x; r[1] = x0.y; r[2] = x1.x; r[3] = x1.y; r[4] = x2.x; r[5] = x2.y; r[6] = x3.x; r[7] = x3.y;
    return r;
}
constexpr int STG = 24576;

typedef unsigned long long u64;
DEV u64 need(v4u v) { return WAITOK(false) ? 0ull : __ballot(min4(v) == 0u); }
#define MLDO(d, v, so, m, off) "s_mov_b64 exec, " m "\n\tbuffer_load_dwordx4 " d ", " v ", %[rs], " so " offen offset:" off " sc1\n\t"
#define MLD(d, v, so, m) MLDO(d, v, so, m, "0")
DEV void poll4(v4u rsv, v4u (&A)[4], unsigned v, unsigned s0, unsigned s1, const u64 (&m)[4]) {
    asm volatile("s_nop 4\n\t" MLD("%0", "%4", "%5", "%7") MLDO("%1", "%4", "%5", "%8", "1024") MLD("%2", "%4", "%6", "%9") MLDO("%3", "%4", "%6", "%10", "1024")
                 "s_mov_b64 exec, -1\n\ts_waitcnt vmcnt(0)"
                 : "+v"(A[0]), "+v"(A[1]), "+v"(A[2]), "+v"(A[3]) : "v"(v), "s"(s0), "s"(s1), "s"(m[0]), "s"(m[1]), "s"(m[2]), "s"(m[3]), [rs] "s"(rsv) : "memory");
}
DEV bool all_nz(unsigned m) { return WAITOK(__all(m != 0u)); }
DEV bool gather_mem(rsrc_t rs, v4u rsv, unsigned par0, int s, int lane, int wave, char* memf, v4u (&A)[4]) {
    const unsigned L = L_MEM + (unsigned)(s & 3) * MEM_SLOT, l16 = lane * 16;
    const unsigned o0 = __builtin_amdgcn_readfirstlane(rmap(L, par0)), o1 = __builtin_amdgcn_readfirstlane(rmap(L + 2048 + wave * 2048, par0));
    bool good = true;
    A[0] = (v4u){0, 0, 0, 0}; A[1] = A[0]; A[2] = A[0]; A[3] = A[0];
    u64 m[4] = {~0ull, ~0ull, ~0ull, ~0ull};
    for (unsigned spins = 0;;) {
        poll4(rsv, A, l16, o0, o1, m);
#pragma unroll
        for (int i = 0; i < 4; ++i) m[i] = need(A[i]);
        if (!(m[0] | m[1] | m[2] | m[3])) break;
        if (spin_fail(rs, spins)) { good = false; break; }
    }
    sts16(memf + (2 + 2 * wave) * 1024 + l16, A[2]); sts16(memf + (3 + 2 * wave) * 1024 + l16, A[3]);
    return good;
}
DEV unsigned wm_off(int s, unsigned L, unsigned par) {
    const unsigned sl = s & 3;
    return sl < 2 ? rmap(L_WM + sl * WM_SLOT + L, par & 1u) : PAGE + rmap((sl - 2) * WM_SLOT + L, par >> 1);
}

DEV unsigned wm_base(int s, unsigned par) { return wm_off(s, 0u, par); }
constexpr unsigned P_FIN = 16384;
constexpr int WOFF = 40960;

constexpr int S_PASS[20] = {0, 1, 2, 3, 4, 5, 6, 7, 8, 9, 10, 12, 14, 16, 18, 11, 13, 15, 17, 19};
constexpr int s_pos(int ms) { for (int i = 0; i < 20; ++i) if (S_PASS[i] == ms) return i; return -1; }

DEV v4u ws_desc(const Params& p) { const u64 a = (u64)p.ws; return (v4u){(unsigned)a, (unsigned)(a >> 32) & 0xffffu, B_END, 0x00020000u}; }
DEV void role_S(const Params& p, rsrc_t rs, int h, char* lds, int t) {
    const v4u rsv = ws_desc(p);
    const int lane = t & 63, wave = __builtin_amdgcn_readfirstlane(t >> 6), n = lane & 15, g = lane >> 4;
    char* const MEMF = lds;
    char* const QK = lds + 20480;
    char* const SPX = lds + 28160;
    char* const WA = lds + WOFF;
    constexpr int SPIN = 31;
    const int efull = (wave & 1) ? 48 * wave + 16 : 48 * wave, ehalf = (wave & 1) ? 48 * wave : 48 * wave + 32;
    v8u W[45];
    {
        unsigned vo[6];
#pragma unroll
        for (int k = 0; k < 6; ++k) {
            const int i = (wave * 6 + k) * 64 + lane, ch = stg_src<384>(i / 24, i % 24);
            vo[k] = (unsigned)((i / 24) * HD * 2 + ch * 16);
        }
        const unsigned ldsb = (unsigned)(size_t)(LAS char*)WA + wave * 6144;
        auto issue = [&](int ps) {
            const uint16_t* src = (S_PASS[ps] < 10 ? p.Wq : p.Wk) + (size_t)(S_PASS[ps] % 10) * 64 * HD + h * DH;
#pragma unroll
            for (int k = 0; k < 6; ++k) dma16(src, vo[k], ldsb + (ps % 5) * STG + k * 1024);
        };
#pragma unroll
        for (int ps = 0; ps < 4; ++ps) issue(ps);
#pragma unroll
        for (int ps = 0; ps < 20; ++ps) {
            if (ps <= 16) vmwait<18>(); else if (ps == 17) vmwait<12>(); else if (ps == 18) vmwait<6>(); else vmwait<0>();
            bar();
            if (ps + 4 < 20) issue(ps + 4);
            const char* tile = WA + (ps % 5) * STG;
            v8u f[3];
#pragma unroll
            for (int tl = 0; tl < 3; ++tl) f[tl] = tr_frag<384>(tile, 0, lane, (tl < 2 ? efull + 16 * tl : ehalf) + 4 * (lane & 3));
            if (ps < 15) {
#pragma unroll
                for (int tl = 0; tl < 3; ++tl) { W[ps * 3 + tl] = f[tl]; if (ps * 3 + tl < SPIN) asm volatile("" : "+a"(W[ps * 3 + tl])); }
            } else {
                bar();
#pragma unroll
                for (int tl = 0; tl < 3; ++tl) {
                    char* d = WA + (ps % 5) * STG + (wave * 3 + tl) * 2048 + lane * 16;
                    sts16(d, lo4(f[tl])); sts16(d + 1024, hi4(f[tl]));
                }
            }
        }
    }
    TS(h, 64, 0);
    unsigned par;
    if (!wait_par(rs, lane, par)) return;
    const unsigned par0 = par & 1u;
    const int idx = (lane & 8) ? 0xEEEE : 0x4444;
    const int bfull = efull >> 5, bhalf = ehalf >> 5;
    const int gh = 2 * (wave & 1) + (g >> 1);
    const float sc2 = 0.07216878364870322f * 1.4426950408889634f;
    v4u kf[2]; v2u kh[2];
    unsigned seenK = 0;
    auto fetch_kx = [&](int s) -> bool {
        if (!wait_head(rs, C_FK, h, s + 1, lane, seenK)) return false;
        const unsigned b = B_KX + (unsigned)(s * NH + h) * 12288u;
#pragma unroll
        for (int kt = 0; kt < 2; ++kt) {
            kf[kt] = ld16s(rs, lane * 16, b + (kt * 6 + bfull) * 1024);
            kh[kt] = ld8s(rs, (gh * 16 + n) * 16 + (g & 1) * 8, b + (kt * 6 + bhalf) * 1024);
        }
        return true;
    };
    const char* const wlb = WA + wave * 6144 + lane * 16;
    auto wl = [&](int i) -> v8u { const char* d = wlb + (i / 3) * STG + (i % 3) * 2048; return cat8(lds16(d), lds16(d + 1024)); };
    constexpr int NWL = 5;
    const int q4 = n >> 2, pp = lane & 3;
    char* const wrF = QK + (efull + n) * 40 + (lane >> 5) * 16 + (g & 1) * 8;
    char* const wrH = QK + (ehalf + n) * 40 + (lane >> 5) * 16 + (g & 1) * 8;
    const char* const trF = QK + (efull + 8 * g + q4) * 40 + pp * 8;
    const char* const trH = QK + (ehalf + 4 * g + q4) * 40 + pp * 8;
    const char* const trAll = QK + (8 * g + q4) * 40 + pp * 8;
    const char* const arot = MEMF + lane * 16;
    char* const spw = SPX + wave * 1408 + (n & 7) * 176;
    const char* const sprd = SPX + (lane >> 3) * 176;
    const unsigned hoff = (h >> 3) * 8192 + (h & 7) * 640;
    const unsigned vp = !(lane & 1) ? (lane >> 3) * 64 + (lane & 6) * 8 : (lane & 7) == 1 ? 512 + (lane >> 3) * 16 : OOB;
    TS(h, 64, 1);
    if (!fetch_kx(0)) return;
    TS(h, 64, 2);
    CYDECL;
    bool good = true;
    for (int s = 0; s < NSEG && good; ++s) {
        CY(0);
        TS(h, s, 0);
        v4f aq[3], ak[3];
#pragma unroll
        for (int tl = 0; tl < 3; ++tl) { aq[tl] = (v4f){0, 0, 0, 0}; ak[tl] = (v4f){0, 0, 0, 0}; }
        late3(aq[0], aq[1], aq[2]); late3(ak[0], ak[1], ak[2]);
        v8u WL[NWL];
#pragma unroll
        for (int i = 0; i < NWL; ++i) WL[i] = wl(i);
        SB();
        v4u A[10];
        auto mm = [&](v4f& acc, int ms, int tl) {
            const int ps = s_pos(ms);
            if (ps * 3 + tl < SPIN) smf_a(acc, A[ms % 10], W[ps * 3 + tl], idx);
            else if (ps < 15) smf_v(acc, A[ms % 10], W[ps * 3 + tl], idx);
            else { const int li = (ps - 15) * 3 + tl; smf_v(acc, A[ms % 10], WL[li % NWL], idx); if (li + NWL < 15) { WL[li % NWL] = wl(li + NWL); SB(); } }
        };
        {
            v4u own[4];
            good = gather_mem(rs, rsv, par0, s, lane, wave, MEMF, own);
            A[0] = own[0]; A[1] = own[1];
        }
        CY(1);
        TS(h, s, 1);
        bar();
        CY(6);
        A[2] = lds16(arot + 2 * 1024); A[3] = lds16(arot + 3 * 1024);
        SB();
#pragma unroll
        for (int j = 0; j < 10; ++j) {
            if (j >= 1 && j + 3 < 10) { A[j + 3] = lds16(arot + (j + 3) * 1024); SB(); }
#pragma unroll
            for (int tl = 0; tl < 3; ++tl) { mm(aq[tl], j, tl); mm(ak[tl], 10 + j, tl); }
        }
        CY(2);
        asm volatile("s_nop 3" : "+v"(aq[0]), "+v"(ak[0]));
        auto comb = [&](int tl) {
            float r[4];
#pragma unroll
            for (int i = 0; i < 4; ++i) { float a = aq[tl][i], b = ak[tl][i]; swap32(a, b); r[i] = a + b; }
            *(LAS v2u*)(tl < 2 ? wrF + tl * 640 : wrH) = (v2u){pk2(r[0], r[1]), pk2(r[2], r[3])};
            SB();
        };
        comb(0);
        late2(aq[1], ak[1]); comb(1);
        {
            const v4u Ff = cat4(trread(trF), trread(trF + 160));
            SB();
            late2(aq[2], ak[2]); comb(2);
            const v2u Fh = trread(trH);
            SB();
            v4f sx0, sx1, sm;
            mf_vv0(sx0, kf[0], Ff); mf_vv0(sx1, kf[1], Ff); mf_vv0(sm, Ff, Ff);
            asm volatile("s_nop 3" ::);
            mf16_vv(sx0, kh[0], Fh); mf16_vv(sx1, kh[1], Fh); mf16_vv(sm, Fh, Fh);
            asm volatile("s_nop 5" : "+v"(sx0), "+v"(sx1), "+v"(sm));
            if (n < 8) {
                *(LAS v4f*)(spw + g * 32) = sx0; *(LAS v4f*)(spw + g * 32 + 16) = sx1;
                if (g >= 2) *(LAS v4f*)(spw + 96 + g * 16) = sm;
            }
        }
        unsigned pslot = rmap(L_P + (unsigned)(s & 3) * P_SLOT + hoff, par0);
        asm volatile("" : "+s"(pslot));
        CY(3);
        bar();
        CY(4);
        TS(h, s, 2);
        if (wave == 0) {
            if (s + 1 < NSEG) {
                v4f xs[4]; float ys[4];
#pragma unroll
                for (int w = 0; w < 4; ++w) { xs[w] = *(const LAS v4f*)(sprd + w * 1408 + (lane & 7) * 16); ys[w] = *(const LAS float*)(sprd + w * 1408 + 128 + (lane & 7) * 4); }
                SB();
                v4f x = (xs[0] + xs[1]) + (xs[2] + xs[3]);
                float y = (ys[0] + ys[1]) + (ys[2] + ys[3]);
                const float ms = max8(fmaxf(fmaxf(fmaxf(x[0], x[1]), fmaxf(x[2], x[3])), y)) * sc2;
#pragma unroll
                for (int i = 0; i < 4; ++i) x[i] = __builtin_amdgcn_exp2f(x[i] * sc2 - ms);
                y = __builtin_amdgcn_exp2f(y * sc2 - ms);
                const float inv = __builtin_amdgcn_rcpf(sum8(((x[0] + x[1]) + (x[2] + x[3])) + y));
                x *= inv; y *= inv;
                const float d0 = __uint_as_float(nz(pk2(x[0], x[1]))), d1 = __uint_as_float(nz(pk2(x[2], x[3])));
                const float dm = __uint_as_float(nz(pk2(dpp<0xB1>(y), y)));
                const float m1 = dpp<0x102>(dm), m2 = dpp<0x104>(dm), m3 = dpp<0x106>(dm);
                const float e0 = dpp<0xB1>(d0), e1 = dpp<0xB1>(d1);
                const bool odd = lane & 1;
                const v4u o = {__float_as_uint(odd ? dm : d0), __float_as_uint(odd ? m1 : d1), __float_as_uint(odd ? m2 : e0), __float_as_uint(odd ? m3 : e1)};
                st16s(rs, vp, pslot, o);
            }
            if (s > 0 && lane < 40) st16s(rs, lane * 16, rmap(L_P + (unsigned)((s - 1) & 3) * P_SLOT + hoff, par0), (v4u){0, 0, 0, 0});
        } else if (wave == 3) {
#pragma unroll
            for (int b = 0; b < 6; ++b) {
                const v4u F = cat4(trread(trAll + b * 1280), trread(trAll + b * 1280 + 160));
                if (n >= 8) st16s(rs, (g * 8 + n - 8) * 16, B_KM + (unsigned)(s * NH + h) * 3072u + b * 512, F);
            }
            post(rs, C_FKM + h * 128, 0, lane, s + 1);
        }
        CY(5);
        TS(h, s, 3);
        if (s + 1 < NSEG) { if (!fetch_kx(s + 1)) return; }
    }
    CYOUT(h);
}

struct VPass { int kind, id, buf, res; };
constexpr int VR0 = WOFF, VR1 = WOFF + 24576, VR2 = WOFF + 49152, VR3 = WOFF + 73728, VR4 = VR3 + 16384, VR5 = VR4 + 16384, VE0 = 0, VE1 = 16384;
constexpr VPass V_PASS[25] = {
    {0, 0, VR0, 0}, {0, 1, VR1, 0}, {0, 2, VR2, 0}, {0, 4, VR3, 0}, {0, 5, VR0, 0}, {0, 7, VR1, 0}, {0, 8, VR2, 0},
    {1, 0, VR5, 0}, {1, 1, VE0, 0}, {1, 2, VE1, 0}, {0, 3, VR0, 1}, {0, 6, VR1, 1}, {0, 9, VR2, 1},
    {1, 3, VR3, 0}, {1, 4, VR4, 0}, {1, 5, VR5, 0}, {1, 6, VE0, 0}, {1, 7, VE1, 0}, {1, 8, VR3, 0}, {1, 9, VR4, 0}, {1, 10, VR5, 0}, {1, 11, VE0, 0},
    {1, 12, VR3, 1}, {1, 13, VR4, 1}, {1, 14, VR5, 1}};
constexpr bool v_overlap(int a, int b) {
    const int a0 = V_PASS[a].buf, a1 = a0 + (V_PASS[a].kind ? 16384 : 24576), b0 = V_PASS[b].buf, b1 = b0 + (V_PASS[b].kind ? 16384 : 24576);
    return a0 < b1 && b0 < a1;
}
struct VTab { int issued[26], wait[25]; };
constexpr VTab v_tab() {
    VTab tb{};
    int j = 0;
    for (int cur = -1; cur < 25; ++cur) {
        for (; j < 25 && j <= cur + 6; ++j) {
            bool ok = true;
            for (int k = cur < 0 ? 0 : cur; k < j; ++k) if (v_overlap(k, j)) ok = false;
            if (!ok) break;
        }
        tb.issued[cur + 1] = j;
    }
    for (int i = 0; i < 25; ++i) { int c = 0; for (int k = i + 1; k < tb.issued[i]; ++k) c += V_PASS[k].kind ? 4 : 6; tb.wait[i] = c; }
    return tb;
}
constexpr VTab V_TAB = v_tab();
static_assert(V_TAB.issued[25] == 25, "load schedule incomplete");
constexpr int v_reg(int kind, int id, int j) {
    if (kind == 0) {
        if (id && id % 3 == 0) return -1 - ((id / 3 - 1) * 3 + j);
        return (id - id / 3) * 3 + j;
    }
    if (id >= 12) return -1 - (9 + (id - 12) * 2 + j);
    return 21 + id * 2 + j;
}
constexpr int v_lds(int i) {
    if (i < 9) return (i / 3 == 0 ? VR0 : i / 3 == 1 ? VR1 : VR2) + (i % 3) * 2048;
    return ((i - 9) / 2 == 0 ? VR3 : (i - 9) / 2 == 1 ? VR4 : VR5) + ((i - 9) & 1) * 2048;
}

DEV void role_V(const Params& p, rsrc_t rs, int h, char* lds, int t) {
    const v4u rsv = ws_desc(p);
    const int lane = t & 63, wave = __builtin_amdgcn_readfirstlane(t >> 6), n = lane & 15, g = lane >> 4;
    char* const MEMF = lds;
    char* const VI = lds + 20480;
    constexpr int VPIN = 31;
    v8u W[45];
    {
        unsigned vo[6], vo2[4];
#pragma unroll
        for (int k = 0; k < 6; ++k) {
            const int i = (wave * 6 + k) * 64 + lane, ch = stg_src<384>(i / 24, i % 24);
            vo[k] = (unsigned)((i / 24) * HD * 2 + ch * 16);
        }
#pragma unroll
        for (int k = 0; k < 4; ++k) { const int i = (wave * 4 + k) * 64 + lane, r = i >> 4, ch = stg_src<256>(r, i & 15); vo2[k] = (unsigned)(r * D * 2 + (ch >> 2) * 320 + (ch & 3) * 16); }
        const unsigned ldsl = (unsigned)(size_t)(LAS char*)lds;
        auto issue = [&](auto ic) {
            constexpr VPass P = V_PASS[decltype(ic)::value];
            if constexpr (P.kind == 0) {
                const uint16_t* src = p.Wv + (size_t)P.id * 64 * HD + h * DH;
#pragma unroll
                for (int k = 0; k < 6; ++k) dma16(src, vo[k], ldsl + P.buf + wave * 6144 + k * 1024);
            } else {
                const uint16_t* src = p.Wo + ((size_t)h * DH + (P.id / 5) * 64) * D + (P.id % 5) * 32;
#pragma unroll
                for (int k = 0; k < 4; ++k) dma16(src, vo2[k], ldsl + P.buf + wave * 4096 + k * 1024);
            }
        };
        sfor<0, V_TAB.issued[0]>(issue);
        sfor<0, 25>([&](auto ic) {
            constexpr int ps = decltype(ic)::value;
            constexpr VPass P = V_PASS[ps];
            constexpr int nf = P.kind ? 2 : 3;
            vmwait<V_TAB.wait[ps]>();
            bar();
            sfor<V_TAB.issued[ps], V_TAB.issued[ps + 1]>(issue);
            const char* tile = lds + P.buf;
            v8u f[nf];
#pragma unroll
            for (int j = 0; j < nf; ++j)
                f[j] = P.kind ? tr_frag<256>(tile, 0, lane, 32 * wave + 16 * j + 4 * (lane & 3)) : tr_frag<384>(tile, 0, lane, 48 * wave + 16 * j + 4 * (lane & 3));
            if constexpr (!P.res) {
#pragma unroll
                for (int j = 0; j < nf; ++j) { const int ri = v_reg(P.kind, P.id, j); W[ri] = f[j]; if (ri < VPIN) asm volatile("" : "+a"(W[ri])); }
            } else {
                bar();
#pragma unroll
                for (int j = 0; j < nf; ++j) { char* d = lds + P.buf + (wave * nf + j) * 2048 + lane * 16; sts16(d, lo4(f[j])); sts16(d + 1024, hi4(f[j])); }
            }
        });
        bar();
    }
    unsigned par;
    if (!wait_par(rs, lane, par)) return;
    const unsigned par0 = par & 1u;
    const int idx = (lane & 8) ? 0xEEEE : 0x4444;
    const char* const gb3 = lds + wave * 6144 + lane * 16;
    const char* const gb2 = lds + wave * 4096 + lane * 16;
    auto gl = [&](int i) -> v8u { const char* d = (i < 9 ? gb3 : gb2) + v_lds(i); return cat8(lds16(d), lds16(d + 1024)); };
    const int q4 = n >> 2, pp = lane & 3;
    char* const wrAB = VI + (48 * wave + (lane < 32 ? 0 : 16) + n) * 16 + (g & 1) * 8;
    char* const wrC = VI + (48 * wave + 32 + n) * 16 + (g & 1) * 8 + (g >> 1) * 4;
    const char* const trV = VI + (16 * g + 4 * (q4 >> 1) + (q4 & 1) + 2 * (pp >> 1)) * 16 + (pp & 1) * 8;
    const char* const arot = MEMF + lane * 16;
    const unsigned vw4 = (2 * (g & 1) + (g >> 1)) * 16384 + n * 16, vw2 = (g >> 1) * 16384 + n * 16;
    TS(32 + h, 64, 1);
    CYDECL;
    bool good = true;
    for (int s = 0; s < NSEG && good; ++s) {
        CY(0);
        TS(32 + h, s, 0);
        v4f av[3], ac[5][2];
#pragma unroll
        for (int tl = 0; tl < 3; ++tl) av[tl] = (v4f){0, 0, 0, 0};
        late3(av[0], av[1], av[2]);
#pragma unroll
        for (int i = 0; i < 5; ++i) { ac[i][0] = (v4f){0, 0, 0, 0}; ac[i][1] = (v4f){0, 0, 0, 0}; late2(ac[i][0], ac[i][1]); }
        v8u G[3];
#pragma unroll
        for (int i = 0; i < 3; ++i) G[i] = gl(i);
        SB();
        const unsigned wmb = wm_base(s, par) + (unsigned)(wave * 10) * 16384u + h * 256;
        auto mm = [&](v4f& acc, v4u a, int kind, int id, int j) {
            const int ri = v_reg(kind, id, j);
            if (ri >= VPIN) smf_v(acc, a, W[ri], idx);
            else if (ri >= 0) smf_a(acc, a, W[ri], idx);
            else { const int li = -1 - ri; smf_v(acc, a, G[li % 3], idx); if (li + 3 < 15) { G[li % 3] = gl(li + 3); SB(); } }
        };
        v4u A[10];
        {
            v4u own[4];
            good = gather_mem(rs, rsv, par0, s, lane, wave, MEMF, own);
            A[0] = own[0]; A[1] = own[1];
        }
        CY(1);
        TS(32 + h, s, 1);
        bar();
        CY(6);
        A[2] = lds16(arot + 2 * 1024); A[3] = lds16(arot + 3 * 1024); A[4] = lds16(arot + 4 * 1024);
        SB();
#pragma unroll
        for (int j = 0; j < 10; ++j) {
            if (j >= 1 && j + 4 < 10) { A[j + 4] = lds16(arot + (j + 4) * 1024); SB(); }
#pragma unroll
            for (int tl = 0; tl < 3; ++tl) mm(av[tl], A[j], 0, j, tl);
        }
        CY(2);
        float r[4];
        asm volatile("s_nop 6" : "+v"(av[0]), "+v"(av[1]));
#pragma unroll
        for (int i = 0; i < 4; ++i) { float a = av[0][i], b = av[1][i]; swap32(a, b); r[i] = a + b; }
        *(LAS v2u*)wrAB = (v2u){pk2(r[0], r[1]), pk2(r[2], r[3])};
        late1(av[2]);
        { float a = av[2][0], b = av[2][2]; swap32(a, b); r[0] = a + b; }
        { float a = av[2][1], b = av[2][3]; swap32(a, b); r[1] = a + b; *(LAS unsigned*)wrC = pk2(r[0], r[1]); }
        bar();
        CY(3);
        v4u VA[3];
#pragma unroll
        for (int ks = 0; ks < 3; ++ks) VA[ks] = cat4(trread(trV + ks * 1024), trread(trV + ks * 1024 + 128));
        unsigned pkd[5][2];
        SB();
        auto half = [&](int pr, int lo) {
            v4f& a0 = ac[pr][0]; v4f& a1 = ac[pr][1];
            float c0, c1;
            { float a = a0[lo], b = a1[lo]; swap32(a, b); c0 = a + b; }
            { float a = a0[lo + 1], b = a1[lo + 1]; swap32(a, b); c1 = a + b; }
            pkd[pr][lo >> 1] = nz(pk2(c0, c1));
            SB();
        };
        auto publish = [&](int q) {
            unsigned x0 = pkd[2 * q][0], x1 = pkd[2 * q][1], y0 = pkd[q < 2 ? 2 * q + 1 : 4][0], y1 = pkd[q < 2 ? 2 * q + 1 : 4][1];
            swap16(x0, y0); swap16(x1, y1);
            if (q < 2 || !(g & 1)) st16s(rs, q < 2 ? vw4 : vw2, wmb + (unsigned)(4 * q) * 16384u, (v4u){x0, x1, y0, y1});
            SB();
        };
#pragma unroll
        for (int pr = 0; pr < 5; ++pr) {
            v4f& a0 = ac[pr][0]; v4f& a1 = ac[pr][1];
            mm(a0, VA[0], 1, pr, 0); mm(a1, VA[0], 1, pr, 1);
            mm(a0, VA[1], 1, 5 + pr, 0); mm(a1, VA[1], 1, 5 + pr, 1);
            mm(a0, VA[2], 1, 10 + pr, 0); mm(a1, VA[2], 1, 10 + pr, 1);
            if (pr > 0) { asm volatile("s_nop 1" : "+v"(ac[pr - 1][0]), "+v"(ac[pr - 1][1])); half(pr - 1, 0); half(pr - 1, 2); }
            if (pr == 2) publish(0);
            if (pr == 4) publish(1);
        }
        late2(ac[4][0], ac[4][1]);
        half(4, 0); half(4, 2);
        publish(2);
        CY(4);
        TS(32 + h, s, 2);
        if (s > 0) {
            const unsigned zb = wm_base(s - 1, par) + (unsigned)(wave * 10) * 16384u + h * 256;
#pragma unroll
            for (int q = 0; q < 3; ++q)
                if (q < 2 || !(g & 1)) st16s(rs, q < 2 ? vw4 : vw2, zb + (unsigned)(4 * q) * 16384u, (v4u){0, 0, 0, 0});
        }
    }
    CYOUT(32 + h);
}

DEV void role_R(const Params& p, rsrc_t rs, int c_, char* lds, int t, unsigned long long litek_) {
    const int c = __builtin_amdgcn_readfirstlane(c_);
    const v4u rsv = ws_desc(p);
    const int lane = t & 63, wave = __builtin_amdgcn_readfirstlane(t >> 6), n = lane & 15, g = lane >> 4;
    char* const RPB = lds;
    unsigned par;
    if (!wait_par(rs, lane, par)) return;
    const unsigned par0 = par & 1u;
    if (t < 16) {
        const int r = t & 7, hf = t >> 3;
        const unsigned* src = (const unsigned*)(p.mem0 + r * D + 16 * c);
        const v4u o = {src[hf], src[hf + 2], src[hf + 4], src[hf + 6]};
        st16(rs, rmap(L_MEM + c * 256 + t * 16, par0), nz4(o));
    }
    const unsigned vpub = lane < 16 ? lane * 16 : OOB;
    const unsigned vex = (n >> 3) * 640 + (n & 7) * 64 + g * 16, vpm = (4 * (n >> 3) + g) * 640 + 512 + (n & 7) * 16;
    unsigned seenU = 0;
    CYDECL;
    bool good = true;
    for (int s = 0; s < NSEG && good; ++s) {
        CY(0);
        TS(64 + c, s, 0);
        v4u z[8];
        if (s + 1 < NSEG) {
            if (!wait_groups(rs, C_FU + wave * 128, 8, s + 1, lane, seenU)) return;
#pragma unroll
            for (int i = 0; i < 8; ++i) z[i] = ld16s(rs, lane * 16, B_ZX + (unsigned)((s * 40 + c) * NH + wave * 8 + i) * 1024u);
        }
        const unsigned op = rmap(L_P + (unsigned)(s & 3) * P_SLOT + wave * 8192, par0), ow = wm_base(s, par) + c * 16384 + wave * 2048;
        const unsigned mnext = __builtin_amdgcn_readfirstlane(rmap(L_MEM + (unsigned)((s + 1) & 3) * MEM_SLOT + c * 256, par0));
        const unsigned mprev = __builtin_amdgcn_readfirstlane(rmap(L_MEM + (unsigned)(s & 3) * MEM_SLOT + c * 256, par0));
        CY(1);
        TS(64 + c, s, 1);
        v4u w0, w1;
        if (s + 1 < NSEG) {
            v4u px[4], pm;
            w0 = (v4u){0, 0, 0, 0}; w1 = w0; pm = w0; px[0] = w0; px[1] = w0; px[2] = w0; px[3] = w0;
            u64 mw0 = ~0ull, mw1 = ~0ull, mm = ~0ull, m0 = ~0ull, m1 = ~0ull, m2 = ~0ull, m3 = ~0ull;
            const unsigned l16 = lane * 16, ops = __builtin_amdgcn_readfirstlane(op), ows = __builtin_amdgcn_readfirstlane(ow);
            for (unsigned spins = 0;;) {
                asm volatile("s_nop 4\n\t" MLD("%0", "%7", "%10", "%12") MLDO("%1", "%7", "%10", "%13", "1024") MLD("%2", "%8", "%11", "%14") MLDO("%3", "%8", "%11", "%15", "1280")
                             MLDO("%4", "%8", "%11", "%16", "2560") MLDO("%5", "%8", "%11", "%17", "3840") MLD("%6", "%9", "%11", "%18") "s_mov_b64 exec, -1\n\ts_waitcnt vmcnt(0)"
                             : "+v"(w0), "+v"(w1), "+v"(px[0]), "+v"(px[1]), "+v"(px[2]), "+v"(px[3]), "+v"(pm)
                             : "v"(l16), "v"(vex), "v"(vpm), "s"(ows), "s"(ops),
                               "s"(mw0), "s"(mw1), "s"(m0), "s"(m1), "s"(m2), "s"(m3), "s"(mm), [rs] "s"(rsv) : "memory");
                if (spins == 0 && s > 0 && lane == 0) st4(rs, C_FW + c * 16 + wave * 4, s);
                mw0 = need(w0); mw1 = need(w1); m0 = need(px[0]); m1 = need(px[1]); m2 = need(px[2]); m3 = need(px[3]); mm = need(pm);
                if (!(mw0 | mw1 | m0 | m1 | m2 | m3 | mm)) break;
                if (spin_fail(rs, spins)) { good = false; break; }
            }
            CY(3);
            TS(64 + c, s, 2);
            v4f aA, aB;
            mf_vv0(aA, w0, pm); mf_vv0(aB, w1, pm);
#pragma unroll
            for (int i = 0; i < 4; ++i) { mf_vv(aA, z[2 * i], px[i]); mf_vv(aB, z[2 * i + 1], px[i]); }
            settle2(aA, aB);
            char* RP = RPB + (s & 1) * 2048;
            {
                float o[4];
#pragma unroll
                for (int i = 0; i < 4; ++i) o[i] = aA[i] + dpp<0x128>(aB[i]);
                if (n < 8) {
                    *(LAS v2u*)(RP + wave * 512 + n * 32 + g * 8) = (v2u){__float_as_uint(o[0]), __float_as_uint(o[1])};
                    *(LAS v2u*)(RP + wave * 512 + (n + 8) * 32 + g * 8) = (v2u){__float_as_uint(o[2]), __float_as_uint(o[3])};
                }
            }
            CY(7);
            bar();
            CY(4);
            if (wave == 0) {
                v4f u = *(const LAS v4f*)(RP + g * 512 + n * 32), v = *(const LAS v4f*)(RP + g * 512 + n * 32 + 16);
                float q[4];
#pragma unroll
                for (int i = 0; i < 4; ++i) { float a = u[i], b = v[i]; swap32(a, b); q[i] = a + b; }
                unsigned a0 = __float_as_uint(q[0]), b0 = __float_as_uint(q[2]), a1 = __float_as_uint(q[1]), b1 = __float_as_uint(q[3]);
                swap16(a0, b0); swap16(a1, b1);
                const unsigned dw = nz(pk2(__uint_as_float(a0) + __uint_as_float(b0), __uint_as_float(a1) + __uint_as_float(b1)));
                unsigned d0 = dw, d1 = dw;
                swap16(d0, d1);
                float f0 = __uint_as_float(d0), f2 = f0, f1 = __uint_as_float(d1), f3 = f1;
                swap32(f0, f2); swap32(f1, f3);
                st16s(rs, vpub, mnext, (v4u){__float_as_uint(f0), __float_as_uint(f1), __float_as_uint(f2), __float_as_uint(f3)});
                CY(5);
                TS(64 + c, s, 3);
            } else if (wave == 1 && lane < 16) st16s(rs, lane * 16, mprev, (v4u){0, 0, 0, 0});
        } else {
            for (unsigned spins = 0;;) {
                w0 = ld16s(rs, lane * 16, ow); w1 = ld16s(rs, lane * 16 + 1024, ow);
                asm volatile("" ::: "memory");
                if (WAITOK(__all(min(min4(w0), min4(w1)) != 0u))) break;
                if (spin_fail(rs, spins)) return;
            }
        }
        st16s(rs, lane * 16, B_ZM + (unsigned)(s * 40 + c) * 8192u + wave * 2048, w0);
        st16s(rs, lane * 16 + 1024, B_ZM + (unsigned)(s * 40 + c) * 8192u + wave * 2048, w1);
        if (s + 2 >= NSEG) post(rs, C_FW + c * 16, wave, lane, s + 1);
    }
    CYOUT(64 + c);
}

DEV void role_U(const Params& p, rsrc_t rs, int h, char* lds, int t) {
    const int lane = t & 63, wave = __builtin_amdgcn_readfirstlane(t >> 6);
    v4u W[60];
    {
        unsigned vo2[4];
#pragma unroll
        for (int k = 0; k < 4; ++k) { const int i = (wave * 4 + k) * 64 + lane, r = i >> 4, ch = stg_src<256>(r, i & 15); vo2[k] = (unsigned)(r * D * 2 + (ch >> 2) * 320 + (ch & 3) * 16); }
        const unsigned ldsl = (unsigned)(size_t)(LAS char*)lds + wave * 4096;
        auto issue = [&](int q) {
            const uint16_t* src = p.Wo + ((size_t)h * DH + (q / 5) * 64) * D + (q % 5) * 32;
#pragma unroll
            for (int k = 0; k < 4; ++k) dma16(src, vo2[k], ldsl + (q % 8) * 16384 + k * 1024);
        };
#pragma unroll
        for (int q = 0; q < 7; ++q) issue(q);
#pragma unroll
        for (int q = 0; q < 15; ++q) {
            vmwait_n(4 * (q + 6 < 15 ? 6 : 14 - q));
            bar();
            if (q + 7 < 15) issue(q + 7);
#pragma unroll
            for (int j = 0; j < 2; ++j) {
                const v8u f = tr_frag<256>(lds + (q % 8) * 16384, 0, lane, 32 * wave + 16 * j + 4 * (lane & 3));
                const int wi = ((q % 5) * 2 + j) * 6 + (q / 5) * 2;
                W[wi] = lo4(f); W[wi + 1] = hi4(f);
                asm volatile("" : "+a"(W[wi]), "+a"(W[wi + 1]));
            }
        }
    }
    TS(176 + h, 64, 0);
    v4u nx[12];
    unsigned seenX = 0;
    auto fetch = [&](int s) -> bool {
        if (!wait_head(rs, C_FX, h, s + 1, lane, seenX)) return false;
#pragma unroll
        for (int k = 0; k < 12; ++k) nx[k] = ld16s(rs, lane * 16, B_VX + (unsigned)(s * NH + h) * 12288u + k * 1024);
        return true;
    };
    if (!fetch(0)) return;
    for (int s = 0; s < NSEG; ++s) {
        TS(176 + h, s, 0);
        v4u vx[12];
#pragma unroll
        for (int k = 0; k < 12; ++k) { vx[k] = nx[k]; asm volatile("" : "+v"(vx[k])); }
        const bool pre = s + 1 < NSEG && seenX >= (unsigned)(s + 2);
        if (pre) { if (!fetch(s + 1)) return; }
        TS(176 + h, s, 1);
        v4f ac[2][2];
        SB();
#pragma unroll
        for (int tl = 0; tl <= 10; ++tl) {
            if (tl < 10) {
                v4f& a0 = ac[tl & 1][0]; v4f& a1 = ac[tl & 1][1];
                mf_va0(a0, vx[0], W[tl * 6]); mf_va0(a1, vx[6], W[tl * 6]);
#pragma unroll
                for (int k = 1; k < 6; ++k) { mf_va(a0, vx[k], W[tl * 6 + k]); mf_va(a1, vx[6 + k], W[tl * 6 + k]); }
            }
            if (tl > 0) {
                v4f& a0 = ac[(tl - 1) & 1][0]; v4f& a1 = ac[(tl - 1) & 1][1];
                if (tl == 10) settle2(a0, a1); else late2(a0, a1);
                const v4u o = {pk2(a0[0], a0[1]), pk2(a0[2], a0[3]), pk2(a1[0], a1[1]), pk2(a1[2], a1[3])};
                st16s(rs, lane * 16, B_ZX + (unsigned)((s * 40 + wave * 10 + tl - 1) * NH + h) * 1024u, o);
                SB();
            }
            if (tl == 5 && s > 8) { vmwait<5>(); if (lane == 0) st4(rs, C_FU + h * 16 + wave * 4, s); SB(); }
        }
        TS(176 + h, s, 2);
        if (s <= 8 || s + 1 == NSEG) post(rs, C_FU + h * 16, wave, lane, s + 1);
        if (!pre && s + 1 < NSEG) { if (!fetch(s + 1)) return; }
    }
}

template <int LD> DEV void load_proj_weights(const uint16_t* wbase, char* lds, int lane, int wave, v4u (&W)[60]) {
    unsigned vo[6];
#pragma unroll
    for (int k = 0; k < 6; ++k) { const int i = (wave * 6 + k) * 64 + lane; vo[k] = (unsigned)((i / 24) * LD * 2 + stg_src<384>(i / 24, i % 24) * 16); }
    const unsigned ldsb = (unsigned)(size_t)(LAS char*)lds + wave * 6144;
    auto issue = [&](int ps) {
        const uint16_t* src = wbase + (size_t)ps * 64 * LD;
#pragma unroll
        for (int k = 0; k < 6; ++k) dma16(src, vo[k], ldsb + (ps % 5) * STG + k * 1024);
    };
    const int pp = lane & 3;
    const int efull = (wave & 1) ? 48 * wave + 16 : 48 * wave, ehalf = (wave & 1) ? 48 * wave : 48 * wave + 32;
#pragma unroll
    for (int ps = 0; ps < 4; ++ps) issue(ps);
#pragma unroll
    for (int ps = 0; ps < 10; ++ps) {
        if (ps <= 6) vmwait<18>(); else if (ps == 7) vmwait<12>(); else if (ps == 8) vmwait<6>(); else vmwait<0>();
        bar();
        if (ps + 4 < 10) issue(ps + 4);
        const char* tile = lds + (ps % 5) * STG;
#pragma unroll
        for (int tl = 0; tl < 3; ++tl) {
            const v8u f = tr_frag<384>(tile, 0, lane, tl == 0 ? efull + 8 * pp : tl == 1 ? efull + 8 * pp + 4 : ehalf + 4 * pp);
            W[ps * 6 + tl] = lo4(f); W[ps * 6 + 3 + tl] = hi4(f);
            asm volatile("" : "+a"(W[ps * 6 + tl]), "+a"(W[ps * 6 + 3 + tl]));
        }
    }
    bar();
}
DEV void proj_mfma(const char* AF, int lane, const v4u (&W)[60], v4f (&acc)[3][2]) {
    v4u b[3][2];
    const char* const a0 = AF + (lane ^ (lane >> 4)) * 16;
    const char* const a1 = AF + (lane ^ (lane >> 4) ^ 4) * 16;
#pragma unroll
    for (int kk = 0; kk < 2; ++kk) { b[kk][0] = lds16((kk & 1 ? a1 : a0) + kk * 1024); b[kk][1] = lds16((kk & 1 ? a1 : a0) + (20 + kk) * 1024); }
#pragma unroll
    for (int kk = 0; kk < 20; ++kk) {
        if (kk + 2 < 20) { b[(kk + 2) % 3][0] = lds16((kk & 1 ? a1 : a0) + (kk + 2) * 1024); b[(kk + 2) % 3][1] = lds16((kk & 1 ? a1 : a0) + (22 + kk) * 1024); }
        SB();
#pragma unroll
        for (int tl = 0; tl < 3; ++tl) {
            if (kk == 0) { mf_av0(acc[tl][0], W[tl], b[0][0]); mf_av0(acc[tl][1], W[tl], b[0][1]); }
            else { mf_av(acc[tl][0], W[kk * 3 + tl], b[kk % 3][0]); mf_av(acc[tl][1], W[kk * 3 + tl], b[kk % 3][1]); }
        }
        SB();
    }
    settle3(acc[0][0], acc[1][0], acc[2][0]); settle3(acc[0][1], acc[1][1], acc[2][1]);
}
DEV void proj_store(rsrc_t rs, unsigned base, int lane, int wave, const v4f (&acc)[3][2]) {
    const int n = lane & 15, g = lane >> 4;
    const int bfull = (wave & 1) ? (3 * wave + 1) / 2 : (3 * wave) / 2, bhalf = (wave & 1) ? (3 * wave - 1) / 2 : (3 * wave) / 2 + 1;
#pragma unroll
    for (int rt = 0; rt < 2; ++rt) {
        const v4u o = {pk2(acc[0][rt][0], acc[0][rt][1]), pk2(acc[0][rt][2], acc[0][rt][3]), pk2(acc[1][rt][0], acc[1][rt][1]), pk2(acc[1][rt][2], acc[1][rt][3])};
        st16(rs, base + (rt * 6 + bfull) * 1024 + lane * 16, o);
    }
    unsigned x0 = pk2(acc[2][0][0], acc[2][0][1]), x1 = pk2(acc[2][0][2], acc[2][0][3]), y0 = pk2(acc[2][1][0], acc[2][1][1]), y1 = pk2(acc[2][1][2], acc[2][1][3]);
    swap16(x0, y0); swap16(x1, y1);
    const v4u o = {x0, x1, y0, y1};
    st16(rs, base + ((g & 1) * 6 + bhalf) * 1024 + ((2 * (wave & 1) + (g >> 1)) * 16 + n) * 16, o);
}
DEV bool fetch_hf(rsrc_t rs, int s, int t, unsigned long long& hmask, v4u (&pf)[10]) {
    for (unsigned spins = 0; !((hmask >> s) & 1ull);) {
        const unsigned f0 = ld4(rs, C_FH + (t & 63) * 4), f1 = ld4(rs, C_FH + 256 + (t & 63) * 4);
        hmask = __ballot(min(f0, f1) != 0u);
        asm volatile("" ::: "memory");
        if (WAITOK((hmask >> s) & 1ull)) break;
        __builtin_amdgcn_s_sleep(POLLSLEEP);
        if (spin_fail(rs, spins)) return false;
    }
#pragma unroll
    for (int k = 0; k < 10; ++k) pf[k] = ld16s(rs, t * 16, B_HF + (unsigned)s * 40960u + k * 4096);
    return true;
}
DEV void put_hf(char* AF, int t, const v4u (&pf)[10]) {
#pragma unroll
    for (int k = 0; k < 10; ++k) sts16(AF + (k * 256 + t) * 16, pf[k]);
}

DEV void role_KX(const Params& p, rsrc_t rs, int j, char* lds, int t) {
    const int lane = t & 63, wave = __builtin_amdgcn_readfirstlane(t >> 6);
    const int jj = j < 24 ? j : j - 24;
    v4u W[80];
    {
        const uint16_t* wbase = (j < 24 ? p.Wk : p.Wv) + jj * 256;
        unsigned vo[8];
#pragma unroll
        for (int k = 0; k < 8; ++k) { const int i = (wave * 8 + k) * 64 + lane; vo[k] = (unsigned)((i >> 5) * HD * 2 + stg_src<512>(i >> 5, i & 31) * 16); }
        const unsigned ldsb = (unsigned)(size_t)(LAS char*)lds + wave * 8192;
        auto issue = [&](int ps) {
            const uint16_t* src = wbase + (size_t)ps * 64 * HD;
#pragma unroll
            for (int k = 0; k < 8; ++k) dma16(src, vo[k], ldsb + (ps % 5) * 32768 + k * 1024);
        };
#pragma unroll
        for (int ps = 0; ps < 4; ++ps) issue(ps);
#pragma unroll
        for (int ps = 0; ps < 10; ++ps) {
            if (ps <= 6) vmwait<24>(); else if (ps == 7) vmwait<16>(); else if (ps == 8) vmwait<8>(); else vmwait<0>();
            bar();
            if (ps + 4 < 10) issue(ps + 4);
#pragma unroll
            for (int tl = 0; tl < 4; ++tl) {
                const v8u f = tr_frag<512>(lds + (ps % 5) * 32768, 0, lane, 64 * wave + 32 * (tl >> 1) + 8 * (lane & 3) + 4 * (tl & 1));
                W[ps * 8 + tl] = lo4(f); W[ps * 8 + 4 + tl] = hi4(f);
                if (ps < 8) asm volatile("" : "+a"(W[ps * 8 + tl]), "+a"(W[ps * 8 + 4 + tl]));
            }
        }
        bar();
    }
    TS(128 + j, 64, 0);
    const unsigned flag = (j < 24 ? C_FK : C_FX) + jj * 16;
    const int b0 = 8 * jj + 2 * wave, b1 = b0 + 1;
    const unsigned ob0 = (j < 24 ? B_KX : B_VX) + (b0 / 6) * 12288 + (b0 % 6) * 1024, ob1 = (j < 24 ? B_KX : B_VX) + (b1 / 6) * 12288 + (b1 % 6) * 1024;
    v4u pf[10];
    unsigned long long hmask = 0;
    if (!fetch_hf(rs, 0, t, hmask, pf)) return;
    put_hf(lds, t, pf);
    bar();
    for (int s = 0; s < NSEG; ++s) {
        TS(128 + j, s, 0);
        if (s + 1 < NSEG) { if (!fetch_hf(rs, s + 1, t, hmask, pf)) return; }
        TS(128 + j, s, 1);
        v4f acc[4][2];
        {
            const char* AF = lds + (s & 1) * 40960;
            const char* const a0 = AF + (lane ^ (lane >> 4)) * 16;
            const char* const a1 = AF + (lane ^ (lane >> 4) ^ 4) * 16;
            v4u b[3][2];
#pragma unroll
            for (int kk = 0; kk < 2; ++kk) { b[kk][0] = lds16((kk & 1 ? a1 : a0) + kk * 1024); b[kk][1] = lds16((kk & 1 ? a1 : a0) + (20 + kk) * 1024); }
#pragma unroll
            for (int kk = 0; kk < 20; ++kk) {
                if (kk + 2 < 20) { b[(kk + 2) % 3][0] = lds16((kk & 1 ? a1 : a0) + (kk + 2) * 1024); b[(kk + 2) % 3][1] = lds16((kk & 1 ? a1 : a0) + (22 + kk) * 1024); }
                SB();
#pragma unroll
                for (int tl = 0; tl < 4; ++tl)
#pragma unroll
                    for (int rt = 0; rt < 2; ++rt) {
                        if (kk == 0) mf_av0(acc[tl][rt], W[tl], b[0][rt]);
                        else if (kk < 16) mf_av(acc[tl][rt], W[kk * 4 + tl], b[kk % 3][rt]);
                        else mf_vv(acc[tl][rt], W[kk * 4 + tl], b[kk % 3][rt]);
                    }
                SB();
            }
            settle2(acc[3][0], acc[3][1]); late3(acc[0][0], acc[1][0], acc[2][0]); late3(acc[0][1], acc[1][1], acc[2][1]);
        }
        TS(128 + j, s, 2);
        if (s + 1 < NSEG) put_hf(lds + ((s + 1) & 1) * 40960, t, pf);
        drain();
        if (s > 8 && lane == 0) st4(rs, flag + wave * 4, s);
#pragma unroll
        for (int bb = 0; bb < 2; ++bb)
#pragma unroll
            for (int rt = 0; rt < 2; ++rt) {
                const v4f x = acc[2 * bb][rt], y = acc[2 * bb + 1][rt];
                st16s(rs, lane * 16, (bb ? ob1 : ob0) + (unsigned)s * (NH * 12288) + rt * 6144, (v4u){pk2(x[0], x[1]), pk2(x[2], x[3]), pk2(y[0], y[1]), pk2(y[2], y[3])});
            }
        TS(128 + j, s, 3);
        if (s <= 8 || s + 1 == NSEG) post(rs, flag, wave, lane, s + 1);
        TS(128 + j, s, 4);
        bar();
    }
}

DEV void role_O(const Params& p, rsrc_t rs, int set, int gam, char* lds, int t) {
    const int lane = t & 63, wave = __builtin_amdgcn_readfirstlane(t >> 6), n = lane & 15, g = lane >> 4;
    const int c = gam * 4 + wave;
    unsigned got;
    if (!wait_word(rs, C_MODB, 40u, got)) return;
    const v4u gau = ld16(rs, B_MOD + (2 * D + 16 * c + 4 * g) * 4);
    unsigned seenU = 0, seenW = 0, seenP = 0;
    for (int s = set; s < NSEG; s += 2) {
        TS(256 + set * 10 + gam, s, 0);
        if (!wait_groups(rs, C_FU, 32, s + 1, lane, seenU)) return;
        v4u z[40];
#pragma unroll
        for (int i = 0; i < 32; ++i) z[i] = ld16s(rs, lane * 16, B_ZX + (unsigned)((s * 40 + c) * NH + i) * 1024u);
        v2u xr[2];
#pragma unroll
        for (int rt = 0; rt < 2; ++rt) xr[rt] = *(const v2u*)(p.x + (size_t)(32 * s + 16 * rt + n) * D + 16 * c + 4 * g);
        TS(256 + set * 10 + gam, s, 1);
        if (!wait_groups(rs, C_FW + c * 16, 1, s + 1, lane, seenW)) return;
        TS(256 + set * 10 + gam, s, 2);
#pragma unroll
        for (int i = 0; i < 8; ++i) z[32 + i] = ld16s(rs, lane * 16, B_ZM + (unsigned)(s * 40 + c) * 8192u + i * 1024);
        if (!wait_groups(rs, C_FP, 32, s + 1, lane, seenP)) return;
        TS(256 + set * 10 + gam, s, 3);
        {
            v4u pf[20];
#pragma unroll
            for (int k = 0; k < 20; ++k) pf[k] = k < 16 ? ld16s(rs, t * 16, B_PX + (unsigned)s * 65536u + k * 4096) : ld16s(rs, t * 16, B_PM + (unsigned)s * 16384u + (k - 16) * 4096);
#pragma unroll
            for (int k = 0; k < 20; ++k) sts16(lds + (k * 256 + t) * 16, pf[k]);
        }
        bar();
        v4f a0 = {0, 0, 0, 0}, a1 = a0;
#pragma unroll
        for (int i = 0; i < 32; ++i) {
            a0 = mfma32(z[i], lds16(lds + i * 1024 + lane * 16), a0);
            a1 = mfma32(z[i], lds16(lds + (32 + i) * 1024 + lane * 16), a1);
        }
#pragma unroll
        for (int i = 0; i < 8; ++i) {
            a0 = mfma32(z[32 + i], lds16(lds + 65536 + i * 1024 + lane * 16), a0);
            a1 = mfma32(z[32 + i], lds16(lds + 65536 + (8 + i) * 1024 + lane * 16), a1);
        }
#pragma unroll
        for (int rt = 0; rt < 2; ++rt) {
            const v4f a = rt ? a1 : a0;
            const float r0 = bflo(xr[rt].x) + rb(__uint_as_float(gau.x) * rb(a[0])), r1 = bfhi(xr[rt].x) + rb(__uint_as_float(gau.y) * rb(a[1]));
            const float r2 = bflo(xr[rt].y) + rb(__uint_as_float(gau.z) * rb(a[2])), r3 = bfhi(xr[rt].y) + rb(__uint_as_float(gau.w) * rb(a[3]));
            st8s(rs, n * 32 + g * 8, B_X1 + (unsigned)(s * 40 + c) * 1024u + rt * 512, (v2u){pk2(r0, r1), pk2(r2, r3)});
        }
        TS(256 + set * 10 + gam, s, 4);
        post(rs, C_FO + (set * 10 + gam) * 16, wave, lane, s + 1);
        TS(256 + set * 10 + gam, s, 5);
        bar();
    }
}

DEV void role_Q(const Params& p, rsrc_t rs, int h, char* lds, int t) {
    const int lane = t & 63, wave = __builtin_amdgcn_readfirstlane(t >> 6), n = lane & 15, g = lane >> 4;
    v4u W[60];
    load_proj_weights<HD>(p.Wq + h * DH, lds, lane, wave, W);
    char* const SPB = lds + 81920;
    const int bfull = (wave & 1) ? (3 * wave + 1) / 2 : (3 * wave) / 2, bhalf = (wave & 1) ? (3 * wave - 1) / 2 : (3 * wave) / 2 + 1;
    const int gh = 2 * (wave & 1) + (g >> 1);
    const float sc2 = 0.07216878364870322f * 1.4426950408889634f;
    v4u pf[10];
    unsigned seenK = 0;
    unsigned long long hmask = 0;
    if (!fetch_hf(rs, 0, t, hmask, pf)) return;
    put_hf(lds, t, pf);
    bar();
    for (int s = 0; s < NSEG; ++s) {
        TS(208 + h, s, 0);
        if (s + 1 < NSEG) { if (!fetch_hf(rs, s + 1, t, hmask, pf)) return; }
        if (!wait_head(rs, C_FK, h, s + 1, lane, seenK)) return;
        const unsigned kb = B_KX + (unsigned)(s * NH + h) * 12288u;
        v4u kf[3]; v2u kh[3];
#pragma unroll
        for (int kt = 0; kt < 2; ++kt) {
            kf[kt] = ld16s(rs, lane * 16, kb + (kt * 6 + bfull) * 1024);
            kh[kt] = ld8s(rs, (gh * 16 + n) * 16 + (g & 1) * 8, kb + (kt * 6 + bhalf) * 1024);
        }
        v4f acc[3][2];
        proj_mfma(lds + (s & 1) * 40960, lane, W, acc);
        if (s + 1 < NSEG) put_hf(lds + ((s + 1) & 1) * 40960, t, pf);
        unsigned got;
        TS(208 + h, s, 1);
        if (!wait_word(rs, C_FKM + h * 128, s + 1, got)) return;
        TS(208 + h, s, 2);
        const unsigned mb = B_KM + (unsigned)(s * NH + h) * 3072u;
        kf[2] = ld16s(rs, (g * 8 + (n & 7)) * 16, mb + bfull * 512);
        kh[2] = ld8s(rs, (gh * 8 + (n & 7)) * 16 + (g & 1) * 8, mb + bhalf * 512);
        char* const SP = SPB + (s & 1) * 24576;
#pragma unroll
        for (int kt = 0; kt < 3; ++kt)
#pragma unroll
            for (int rt = 0; rt < 2; ++rt) {
                const v4u qf = {pk2(acc[0][rt][0], acc[0][rt][1]), pk2(acc[0][rt][2], acc[0][rt][3]), pk2(acc[1][rt][0], acc[1][rt][1]), pk2(acc[1][rt][2], acc[1][rt][3])};
                const v2u qh = {pk2(acc[2][rt][0], acc[2][rt][1]), pk2(acc[2][rt][2], acc[2][rt][3])};
                const v4f z4 = {0, 0, 0, 0};
                *(LAS v4f*)(SP + ((wave * 6 + rt * 3 + kt) * 64 + lane) * 16) = mfma32(kf[kt], qf, z4) + mfma16(kh[kt], qh, z4);
            }
        bar();
        if (wave < 2) {
            const int rt = wave;
            v4f sv[3];
#pragma unroll
            for (int kt = 0; kt < 3; ++kt) {
                sv[kt] = (v4f){0, 0, 0, 0};
#pragma unroll
                for (int w = 0; w < 4; ++w) sv[kt] += *(const LAS v4f*)(SP + ((w * 6 + rt * 3 + kt) * 64 + lane) * 16);
            }
#pragma unroll
            for (int kt = 0; kt < 3; ++kt)
#pragma unroll
                for (int i = 0; i < 4; ++i) sv[kt][i] = rb(rb(sv[kt][i]) * 0.07216878364870322f);
            const int tok = 16 * rt + n;
            const float NEG = -3.0e38f;
            float m = NEG;
#pragma unroll
            for (int i = 0; i < 4; ++i) {
                if (4 * g + i > tok) sv[0][i] = NEG;
                if (16 + 4 * g + i > tok) sv[1][i] = NEG;
                if (g >= 2) sv[2][i] = NEG;
                m = fmaxf(m, fmaxf(sv[0][i], fmaxf(sv[1][i], sv[2][i])));
            }
            m = xr16max(m); { float a = m, b = m; swap32(a, b); m = fmaxf(a, b); }
            float sum = 0;
#pragma unroll
            for (int kt = 0; kt < 3; ++kt)
#pragma unroll
                for (int i = 0; i < 4; ++i) { const float e = sv[kt][i] == NEG ? 0.f : __builtin_amdgcn_exp2f((sv[kt][i] - m) * 1.4426950408889634f); sv[kt][i] = e; sum += e; }
            sum = xr16sum(sum); { float a = sum, b = sum; swap32(a, b); sum = a + b; }
            const float inv = 1.0f / sum;
            const v4u o = {pk2(sv[0][0] * inv, sv[0][1] * inv), pk2(sv[0][2] * inv, sv[0][3] * inv), pk2(sv[1][0] * inv, sv[1][1] * inv), pk2(sv[1][2] * inv, sv[1][3] * inv)};
            st16s(rs, lane * 16, B_PX + (unsigned)((s * 2 + rt) * NH + h) * 1024u, o);
            unsigned a0 = pk2(sv[2][0] * inv, sv[2][1] * inv), a1 = pk2(sv[2][2] * inv, sv[2][3] * inv), b0 = a0, b1 = a1;
            swap16(a0, b0); swap16(a1, b1);
            if (lane < 16) st16s(rs, n * 16, B_PM + (unsigned)((s * 2 + rt) * NH + h) * 256u, (v4u){a0, a1, b0, b1});
        }
        TS(208 + h, s, 3);
        post(rs, C_FP + h * 16, wave, lane, s + 1);
        TS(208 + h, s, 4);
    }
}

template <bool EXACT, class LD> DEV void ln_norm(LD ld, int lane, int wave, float (&f)[40]) {
    const int l16 = lane & 15, row = wave * 4 + (lane >> 4);
    v4u v[5];
#pragma unroll
    for (int i = 0; i < 5; ++i) v[i] = ld(row, l16 + 16 * i);
    float sum = 0;
#pragma unroll
    for (int i = 0; i < 5; ++i)
#pragma unroll
        for (int j = 0; j < 4; ++j) { f[i * 8 + 2 * j] = bflo(v[i][j]); f[i * 8 + 2 * j + 1] = bfhi(v[i][j]); sum += f[i * 8 + 2 * j] + f[i * 8 + 2 * j + 1]; }
    const float mean = sum16(sum) * (1.0f / D);
    float q = 0;
#pragma unroll
    for (int i = 0; i < 40; ++i) { const float d = f[i] - mean; q += d * d; }
    q = sum16(q);
    const float mb = rb(mean), sd = rb(sqrtf(rb(rb(q * (1.0f / D)) + 1e-5f)));
    const float inv = 1.0f / sd;
#pragma unroll
    for (int i = 0; i < 40; ++i) f[i] = EXACT ? rb(rb(f[i] - mb) * inv) : (f[i] - mb) * inv;
}
DEV void ln_coef(rsrc_t rs, unsigned shift_off, unsigned scale_off, int lane, float (&sh)[40], float (&sc)[40]) {
    const int l16 = lane & 15;
#pragma unroll
    for (int i = 0; i < 5; ++i)
#pragma unroll
        for (int q = 0; q < 2; ++q) {
            const v4u a = ld16(rs, shift_off + ((l16 + 16 * i) * 8 + q * 4) * 4), b = ld16(rs, scale_off + ((l16 + 16 * i) * 8 + q * 4) * 4);
#pragma unroll
            for (int j = 0; j < 4; ++j) { sh[i * 8 + q * 4 + j] = __uint_as_float(a[j]); sc[i * 8 + q * 4 + j] = rb(1.0f + __uint_as_float(b[j])); }
        }
}
template <bool EXACT> DEV void ln_apply(char* AF, int lane, int wave, const float (&f)[40], const float (&sh)[40], const float (&sc)[40]) {
    const int l16 = lane & 15, row = wave * 4 + (lane >> 4);
#pragma unroll
    for (int i = 0; i < 5; ++i) {
        v4u o;
#pragma unroll
        for (int j = 0; j < 4; ++j) {
            const int e = i * 8 + 2 * j;
            o[j] = EXACT ? pk2(rb(f[e] * sc[e]) + sh[e], rb(f[e + 1] * sc[e + 1]) + sh[e + 1]) : pk2(f[e] * sc[e] + sh[e], f[e + 1] * sc[e + 1] + sh[e + 1]);
        }
        const int ch = l16 + 16 * i;
        sts16(AF + ((ch >> 2) * 64 + (((ch & 3) * 16 + row) ^ (ch & 7))) * 16, o);
    }
}
template <int NC> DEV void mod_task(const Params& p, rsrc_t rs, int col0, unsigned counter, char* lds, int t) {
    constexpr int KG = 256 / NC, KP = D / KG;
    const int cc = t % NC, kg = t / NC;
    float acc[8] = {0, 0, 0, 0, 0, 0, 0, 0};
    v4u w[KP];
#pragma unroll
    for (int k = 0; k < KP; ++k) w[k] = *(const v4u*)(p.wmod + (size_t)(kg * KP + k) * (6 * D) + col0 + cc * 8);
#pragma unroll
    for (int k = 0; k < KP; ++k) {
        const float xc = __uint_as_float((unsigned)p.c[kg * KP + k] << 16);
        const float sl = rb(xc / (1.0f + __expf(-xc)));
#pragma unroll
        for (int j = 0; j < 4; ++j) { acc[2 * j] += sl * bflo(w[k][j]); acc[2 * j + 1] += sl * bfhi(w[k][j]); }
    }
    LAS float* red = (LAS float*)lds;
#pragma unroll
    for (int j = 0; j < 8; ++j) red[kg * (NC * 8 + 1) + cc * 8 + j] = acc[j];
    bar();
    if (t < 64) {
        if (t < NC * 8) {
            float a = 0;
#pragma unroll 8
            for (int k = 0; k < KG; ++k) a += red[k * (NC * 8 + 1) + t];
            st4(rs, B_MOD + (col0 + t) * 4, __float_as_uint(rb(a)));
        }
        drain();
        if (t == 0) __hip_atomic_fetch_add((GAS unsigned*)(p.ws + counter), 1u, __ATOMIC_RELAXED, __HIP_MEMORY_SCOPE_AGENT);
    }
    bar();
}
DEV bool prologue(const Params& p, rsrc_t rs, int pw, char* lds, int t) {
    const int lane = t & 63, wave = __builtin_amdgcn_readfirstlane(t >> 6);
    float sh[40], sc[40];
    for (int task = pw; task < 128; task += 48) {
        float f[40];
        const uint16_t* xs = p.x + (size_t)task * 16 * D;
        ln_norm<true>([&](int row, int ch) { return *(const v4u*)(xs + row * D + ch * 8); }, lane, wave, f);
        if (task == pw) {
            unsigned got;
            TS(256 + (pw & 31), 64, 0 + 4 * (pw >> 5));
            if (!wait_word(rs, C_MODA, 40u, got)) return false;
            TS(256 + (pw & 31), 64, 1 + 4 * (pw >> 5));
            ln_coef(rs, B_MOD, B_MOD + D * 4, lane, sh, sc);
        }
        ln_apply<true>(lds, lane, wave, f, sh, sc);
        bar();
#pragma unroll
        for (int k = 0; k < 5; ++k) st16s(rs, t * 16, B_HF + (unsigned)task * 20480u + k * 4096, lds16(lds + (k * 256 + t) * 16));
        drain();
        bar();
        if (t == 0) st4(rs, C_FH + (task & 1) * 256 + (task >> 1) * 4, 1u);
        if (task == pw) TS(256 + (pw & 31), 64, 2 + 4 * (pw >> 5));
    }
    if (pw < 40) mod_task<8>(p, rs, 2 * D + pw * 64, C_MODB, lds, t);
    return true;
}

DEV void helper(const Params& p, rsrc_t rs, int x, int i, char* lds, int t) {
    const int lane = t & 63, wave = __builtin_amdgcn_readfirstlane(t >> 6);
    if (i < 10) mod_task<4>(p, rs, (x * 10 + i) * 32, C_MODA, lds, t);
    const bool prober = x == 0 && i < 4;
    if (prober && wave == 3) {
        if (lane == 0) {
            unsigned best = 0xffffffffu;
            for (int r = 0; r < 10; ++r) {
                GAS unsigned* a = (GAS unsigned*)(p.ws + ((i >> 1) ? PAGE + rmap(L_PROBE1, i & 1) : rmap(L_PROBE0, i & 1)) + (r & 3) * 2048);
                unsigned z = 0, v; unsigned long long t0, t1;
                asm volatile("s_memtime %1\n\ts_waitcnt lgkmcnt(0)\n\tglobal_atomic_add %0, %3, %4, off sc0 sc1\n\ts_waitcnt vmcnt(0)\n\ts_memtime %2\n\ts_waitcnt lgkmcnt(0)"
                             : "=&v"(v), "=&s"(t0), "=&s"(t1) : "v"(a), "v"(z) : "memory");
                best = min(best, (unsigned)(t1 - t0));
            }
            st4(rs, C_PAR + i * 4, best | 1u);
        }
        return;
    }
    const int nw = prober ? 3 : 4, h = 8 * x + (i & 7);
    v4u sink = {0, 0, 0, 0};
    if (i < 8) {
        for (int q = wave; q < 240; q += nw) {
            const int c = q * 64 + lane, ps = c / 1536, r = (c % 1536) / 24;
            const int ks = ps < 5 ? 2 * ps : 2 * ps - 9;
            const char* a = (const char*)(p.Wk + (size_t)(ks * 64 + r) * HD + h * DH) + (c % 24) * 16;
            asm volatile("global_load_dwordx4 %0, %1, off" : "+v"(sink) : "v"(a) : "memory");
        }
    } else {
        for (int q = wave; q < 240; q += nw) {
            const char* a = (const char*)(p.Wo + (size_t)h * DH * D) + (q * 64 + lane) * 16;
            asm volatile("global_load_dwordx4 %0, %1, off" : "+v"(sink) : "v"(a) : "memory");
        }
    }
    asm volatile("s_waitcnt vmcnt(0)" : "+v"(sink) :: "memory");
}

DEV void role_L(const Params& p, rsrc_t rs, int rt, char* lds, int t) {
    const int lane = t & 63, wave = __builtin_amdgcn_readfirstlane(t >> 6);
    unsigned got;
    if (!wait_word(rs, C_MODB, 40u, got)) return;
    float sh[40], sc[40];
    ln_coef(rs, B_MOD + 3 * D * 4, B_MOD + 4 * D * 4, lane, sh, sc);
    unsigned seen0 = 0, seen1 = 0;
    for (int s = 0; s < NSEG; ++s) {
        TS(300 + rt, s, 0);
        unsigned sn = (s & 1) ? seen1 : seen0;
        if (!wait_groups(rs, C_FO + (s & 1) * 160, 10, s + 1, lane, sn)) return;
        if (s & 1) seen1 = sn; else seen0 = sn;
        TS(300 + rt, s, 1);
        const unsigned xb = B_X1 + (unsigned)s * 40960u + rt * 512;
        float f[40];
        ln_norm<false>([&](int row, int ch) { return ld16s(rs, (ch >> 1) * 1024 + row * 32 + (ch & 1) * 16, xb); }, lane, wave, f);
        char* AF = lds + (s & 1) * 20480;
        ln_apply<false>(AF, lane, wave, f, sh, sc);
        bar();
        TS(300 + rt, s, 2);
#pragma unroll
        for (int k = 0; k < 5; ++k) st16s(rs, t * 16, B_H2 + (unsigned)(s * 2 + rt) * 20480u + k * 4096, lds16(AF + (k * 256 + t) * 16));
        post(rs, C_FL + rt * 16, wave, lane, s + 1);
        TS(300 + rt, s, 3);
    }
}

DEV void role_M1(const Params& p, rsrc_t rs, int phi, char* lds, int t) {
    const int lane = t & 63, wave = __builtin_amdgcn_readfirstlane(t >> 6), g = lane >> 4;
    v4u W[60];
    load_proj_weights<FF>(p.w1 + phi * 192, lds, lane, wave, W);
    const int efull = (wave & 1) ? 48 * wave + 16 : 48 * wave, ehalf = (wave & 1) ? 48 * wave : 48 * wave + 32;
    float bias[3][4];
#pragma unroll
    for (int i = 0; i < 4; ++i) {
        bias[0][i] = __uint_as_float((unsigned)p.b1[phi * 192 + efull + 8 * g + i] << 16);
        bias[1][i] = __uint_as_float((unsigned)p.b1[phi * 192 + efull + 8 * g + 4 + i] << 16);
        bias[2][i] = __uint_as_float((unsigned)p.b1[phi * 192 + ehalf + 4 * g + i] << 16);
    }
    unsigned seenL = 0;
    for (int s = 0; s < NSEG; ++s) {
        TS(280 + phi, s, 0);
        if (!wait_groups(rs, C_FL, 2, s + 1, lane, seenL)) return;
        TS(280 + phi, s, 1);
        char* AF = lds + (s & 1) * 40960;
        {
            v4u pf[10];
#pragma unroll
            for (int k = 0; k < 10; ++k) pf[k] = ld16s(rs, t * 16, B_H2 + (unsigned)s * 40960u + k * 4096);
            put_hf(AF, t, pf);
        }
        bar();
        TS(280 + phi, s, 2);
        v4f acc[3][2];
        proj_mfma(AF, lane, W, acc);
        TS(280 + phi, s, 3);
#pragma unroll
        for (int tl = 0; tl < 3; ++tl)
#pragma unroll
            for (int rt = 0; rt < 2; ++rt)
#pragma unroll
                for (int i = 0; i < 4; ++i) { float v = rb(rb(acc[tl][rt][i]) + bias[tl][i]); v = v > 0.f ? v : 0.f; acc[tl][rt][i] = v * v; }
        proj_store(rs, B_HID + (unsigned)(s * 8 + phi) * 12288u, lane, wave, acc);
        post(rs, C_FM1 + phi * 16, wave, lane, s + 1);
        TS(280 + phi, s, 4);
    }
}

DEV void role_M2(const Params& p, rsrc_t rs, int gam, char* lds, int t) {
    const int lane = t & 63, wave = __builtin_amdgcn_readfirstlane(t >> 6), n = lane & 15, g = lane >> 4;
    v4u W[48];
    {
        unsigned vo[6];
#pragma unroll
        for (int k = 0; k < 6; ++k) { const int i = (wave * 6 + k) * 64 + lane; vo[k] = (unsigned)((i >> 3) * D * 2 + stg_src<128>(i >> 3, i & 7) * 16); }
        const unsigned ldsb = (unsigned)(size_t)(LAS char*)lds + wave * 6144;
        auto issue = [&](int ps) {
            const uint16_t* src = p.w2 + (size_t)ps * 192 * D + gam * 64;
#pragma unroll
            for (int k = 0; k < 6; ++k) dma16(src, vo[k], ldsb + (ps % 5) * STG + k * 1024);
        };
#pragma unroll
        for (int ps = 0; ps < 4; ++ps) issue(ps);
#pragma unroll
        for (int ps = 0; ps < 8; ++ps) {
            if (ps <= 4) vmwait<18>(); else if (ps == 5) vmwait<12>(); else if (ps == 6) vmwait<6>(); else vmwait<0>();
            bar();
            if (ps + 4 < 8) issue(ps + 4);
            const char* tile = lds + (ps % 5) * STG;
#pragma unroll
            for (int j = 0; j < 3; ++j) {
                const v8u f = tr_frag<128>(tile, 64 * j, lane, 16 * wave + 4 * (lane & 3));
                W[(ps * 3 + j) * 2] = lo4(f); W[(ps * 3 + j) * 2 + 1] = hi4(f);
                asm volatile("" : "+a"(W[(ps * 3 + j) * 2]), "+a"(W[(ps * 3 + j) * 2 + 1]));
            }
        }
        bar();
    }
    const int col = gam * 64 + wave * 16 + 4 * g;
    const v2u b2u = *(const v2u*)(p.b2 + col);
    unsigned got;
    if (!wait_word(rs, C_MODB, 40u, got)) return;
    const v4u gbu = ld16(rs, B_MOD + (5 * D + col) * 4);
    unsigned seenM = 0;
    for (int s = 0; s < NSEG; ++s) {
        TS(290 + gam, s, 0);
        if (!wait_groups(rs, C_FM1, 8, s + 1, lane, seenM)) return;
        TS(290 + gam, s, 1);
        v2u xr[2];
#pragma unroll
        for (int rt = 0; rt < 2; ++rt) xr[rt] = ld8s(rs, (16 * rt + n) * 32 + g * 8, B_X1 + (unsigned)(s * 40 + gam * 4 + wave) * 1024u);
#pragma unroll
        for (int half = 0; half < 2; ++half) {
            v4u pf[12];
#pragma unroll
            for (int k = 0; k < 12; ++k) pf[k] = ld16s(rs, t * 16, B_HID + (unsigned)s * 98304u + (half * 12 + k) * 4096);
#pragma unroll
            for (int k = 0; k < 12; ++k) sts16(lds + ((half * 12 + k) * 256 + t) * 16, pf[k]);
        }
        bar();
        TS(290 + gam, s, 2);
        v4f a0, a1;
        v4u b[6][2];
        auto fb = [&](int kk) { return lds + ((kk / 6) * 12 + (kk % 6)) * 1024 + lane * 16; };
#pragma unroll
        for (int kk = 0; kk < 5; ++kk) { b[kk][0] = lds16(fb(kk)); b[kk][1] = lds16(fb(kk) + 6144); }
#pragma unroll
        for (int kk = 0; kk < 48; ++kk) {
            if (kk + 5 < 48) { b[(kk + 5) % 6][0] = lds16(fb(kk + 5)); b[(kk + 5) % 6][1] = lds16(fb(kk + 5) + 6144); }
            SB();
            if (kk == 0) { mf_av0(a0, W[0], b[0][0]); mf_av0(a1, W[0], b[0][1]); }
            else { mf_av(a0, W[kk], b[kk % 6][0]); mf_av(a1, W[kk], b[kk % 6][1]); }
            SB();
        }
        settle2(a0, a1);
#pragma unroll
        for (int rt = 0; rt < 2; ++rt) {
            const v4f a = rt ? a1 : a0;
            const float r0 = bflo(xr[rt].x) + rb(__uint_as_float(gbu.x) * rb(rb(a[0]) + bflo(b2u.x))), r1 = bfhi(xr[rt].x) + rb(__uint_as_float(gbu.y) * rb(rb(a[1]) + bfhi(b2u.x)));
            const float r2 = bflo(xr[rt].y) + rb(__uint_as_float(gbu.z) * rb(rb(a[2]) + bflo(b2u.y))), r3 = bfhi(xr[rt].y) + rb(__uint_as_float(gbu.w) * rb(rb(a[3]) + bfhi(b2u.y)));
            *(v2u*)(p.out + (size_t)(32 * s + 16 * rt + n) * D + col) = (v2u){pk2(r0, r1), pk2(r2, r3)};
        }
        TS(290 + gam, s, 3);
        bar();
    }
}

__global__ void __launch_bounds__(256, 1) fused_kernel(Params p) {
    extern __shared__ __attribute__((aligned(16))) char lds[];
    const int t = threadIdx.x;
    const unsigned long long litek_ = 0;
    const rsrc_t rs = __builtin_amdgcn_make_buffer_rsrc(p.ws, 0, B_END, 0x00020000);
    if (t < 64) {
        const unsigned x = (unsigned)__builtin_amdgcn_s_getreg(20 | (3 << 11)) & 7u;
        unsigned k = 0;
        if (t == 0) k = __hip_atomic_fetch_add((GAS unsigned*)(p.ws + C_TICK + x * 128), 1u, __ATOMIC_RELAXED, __HIP_MEMORY_SCOPE_AGENT);
        k = __builtin_amdgcn_readfirstlane(k);
        unsigned slot = x * 32 + k;
        if (k >= 32) {
            unsigned cnt[8];
            for (unsigned spins = 0;;) {
                const unsigned v = ld4(rs, C_TICK + (t & 7) * 128);
                asm volatile("" ::: "memory");
                unsigned tot = 0;
#pragma unroll
                for (int y = 0; y < 8; ++y) { cnt[y] = __builtin_amdgcn_readlane(v, y); tot += cnt[y]; }
                if (tot >= 256u || spin_fail(rs, spins)) break;
            }
            unsigned rank = k - 32;
#pragma unroll
            for (int y = 0; y < 8; ++y) if ((unsigned)y < x && cnt[y] > 32u) rank += cnt[y] - 32u;
            slot = 255;
#pragma unroll
            for (int y = 7; y >= 0; --y) {
                unsigned before = 0;
#pragma unroll
                for (int z = 0; z < 8; ++z) if (z < y && cnt[z] < 32u) before += 32u - cnt[z];
                if (cnt[y] < 32u && rank >= before && rank - before < 32u - cnt[y]) slot = y * 32 + cnt[y] + (rank - before);
            }
        }
        if (t == 0) *(LAS unsigned*)lds = slot;
    }
    bar();
    const int slot = __builtin_amdgcn_readfirstlane((int)*(LAS unsigned*)lds) & 255;
    const int sx = slot >> 5, sk = slot & 31;
    int role = sx < 4 ? (sk < 8 ? 8 * sx + sk : sk < 16 ? 32 + 8 * sx + (sk - 8) : sk < 26 ? 64 + 10 * sx + (sk - 16) : 104 + 6 * sx + (sk - 26))
                      : (sk < 12 ? 136 + 12 * (sx - 4) + sk : sk < 20 ? 184 + 8 * (sx - 4) + (sk - 12) : sk < 22 ? 128 + 2 * (sx - 4) + (sk - 20) : 216 + 4 * (sk - 22) + (sx - 4));
    bar();
    TS(role, 65, 0);
    if (role >= 64 && role < 128) helper(p, rs, sx, sk - 16, lds, t);
    if (role >= 216 || (role >= 128 && role < 136)) {
        if (!prologue(p, rs, role >= 216 ? role - 216 : role - 88, lds, t)) return;
        bar();
    }
    if (role < 32) role_S(p, rs, role, lds, t);
    else if (role < 64) role_V(p, rs, role - 32, lds, t);
    else if (role < 104) role_R(p, rs, role - 64, lds, t, litek_);
    else if (role < 136) role_Q(p, rs, role - 104, lds, t);
    else if (role < 184) role_KX(p, rs, role - 136, lds, t);
    else if (role < 216) role_U(p, rs, role - 184, lds, t);
    else if (role < 236) role_O(p, rs, (role - 216) & 1, (role - 216) >> 1, lds, t);
    else if (role < 244) role_M1(p, rs, role - 236, lds, t);
    else if (role < 254) role_M2(p, rs, role - 244, lds, t);
    else role_L(p, rs, role - 254, lds, t);
}

extern "C" void kernel_launch(void* const* d_in, const int* in_sizes, int n_in, void* d_out, int out_size, void* d_ws, size_t ws_size, hipStream_t stream) {
    Params p;
    p.x = (const uint16_t*)d_in[0]; p.mem0 = (const uint16_t*)d_in[1];
    p.Wq = (const uint16_t*)d_in[2]; p.Wk = (const uint16_t*)d_in[3]; p.Wv = (const uint16_t*)d_in[4]; p.Wo = (const uint16_t*)d_in[5];
    p.w1 = (const uint16_t*)d_in[8]; p.b1 = (const uint16_t*)d_in[9]; p.w2 = (const uint16_t*)d_in[10]; p.b2 = (const uint16_t*)d_in[11];
    p.c = (const uint16_t*)d_in[12]; p.wmod = (const uint16_t*)d_in[13];
    p.out = (uint16_t*)d_out;
    p.ws = (unsigned char*)(((uintptr_t)d_ws + PAGE - 1) & ~(uintptr_t)(PAGE - 1));
    (void)hipFuncSetAttribute((const void*)fused_kernel, hipFuncAttributeMaxDynamicSharedMemorySize, LDS_BYTES);
    (void)hipMemsetAsync(p.ws, 0, ZERO_BYTES, stream);
#define SOLO_GRID 256
    hipLaunchKernelGGL(fused_kernel, dim3(SOLO_GRID), dim3(256), LDS_BYTES, stream, p);
}
```

```cpp
#include <hip/hip_runtime.h>
#include <hip/hip_bf16.h>
#include <stdint.h>

#define LAS __attribute__((address_space(3)))
#define GAS __attribute__((address_space(1)))
#define DEV __device__ __forceinline__
typedef unsigned v4u __attribute__((ext_vector_type(4)));
typedef unsigned v2u __attribute__((ext_vector_type(2)));
typedef unsigned v8u __attribute__((ext_vector_type(8)));
typedef float v4f __attribute__((ext_vector_type(4)));
typedef short v4s __attribute__((ext_vector_type(4)));
typedef __bf16 bf8 __attribute__((ext_vector_type(8)));
typedef __bf16 bf16v __attribute__((ext_vector_type(16)));
typedef __bf16 bf2 __attribute__((ext_vector_type(2)));
typedef __amdgpu_buffer_rsrc_t rsrc_t;

constexpr int D = 640, NH = 32, DH = 192, HD = NH * DH, NSEG = 64, FF = 1536;

constexpr unsigned PAGE = 2u << 20;
constexpr unsigned C_ABORT = 512, C_PAR = 768, C_FH = 4096  , C_MODA = 1024, C_TICK = 2048;
#define C_FK (fb + 512)
#define C_FX (fb + 1024)
#define C_FU (fb + 1536)
#define C_FP (fb + 2048)
#define C_FM1 (fb + 2560)
#define C_OC (fb + 2816)
#define C_FW (fb + 3072)
#define C_FL (fb + 3840)
#define C_OTK (fb + 3968)
#define C_MODB (fb + 4032)
#define C_FKM (fb + 4096)
constexpr unsigned L_MEM = 8192, MEM_SLOT = 16384;
constexpr unsigned L_P = L_MEM + 4 * MEM_SLOT, P_SLOT = 32768;
constexpr unsigned L_WM = L_P + 4 * P_SLOT, WM_SLOT = 327680;
constexpr unsigned L_FLAGS = L_WM + 2 * WM_SLOT + 8192;
constexpr unsigned L_PROBE0 = L_WM + 2 * WM_SLOT, L_PROBE1 = 2 * WM_SLOT;
constexpr unsigned ZERO_BYTES = PAGE + 2 * (L_PROBE1 + 8192);
constexpr unsigned B_MOD = 2 * PAGE;
constexpr unsigned B_HF = B_MOD + 16384;
constexpr unsigned B_KX = B_HF + NSEG * 40960;
constexpr unsigned B_VX = B_KX + NSEG * NH * 12288;
constexpr unsigned B_KM = B_VX + NSEG * NH * 12288;
constexpr unsigned B_ZX = B_KM + NSEG * NH * 3072;
constexpr unsigned B_ZM = B_ZX + NSEG * 40 * NH * 1024;
constexpr unsigned B_PX = B_ZM + NSEG * 40 * 8192;
constexpr unsigned B_PM = B_PX + NSEG * NH * 2048;
constexpr unsigned B_X1 = B_PM + NSEG * 16384;
constexpr unsigned B_HID = B_X1 + 2048 * D * 2;
constexpr unsigned B_H2 = B_HID + NSEG * 8 * 12288;
constexpr unsigned B_END = B_H2 + NSEG * 40960;

constexpr unsigned OOB = 0x80000000u;
constexpr int LDS_BYTES = 160 * 1024;
constexpr unsigned SPIN_CAP = 1u << 18;
#define OPRE 50
#define QR 15
#define QV 16
#define POLLSLEEP 8

struct Params {
    const uint16_t* x; const uint16_t* mem0; const uint16_t* Wq; const uint16_t* Wk; const uint16_t* Wv; const uint16_t* Wo;
    const uint16_t* w1; const uint16_t* b1; const uint16_t* w2; const uint16_t* b2; const uint16_t* c; const uint16_t* wmod;
    uint16_t* out; unsigned char* ws;
};

template <int V> struct IC { static constexpr int value = V; };
template <int I, int N, class F> DEV void sfor(F&& f) { if constexpr (I < N) { f(IC<I>{}); sfor<I + 1, N>(f); } }

DEV v4u ld16(rsrc_t r, unsigned off) { return __builtin_amdgcn_raw_buffer_load_b128(r, off, 0, 16); }
DEV unsigned ld4(rsrc_t r, unsigned off) { return __builtin_amdgcn_raw_buffer_load_b32(r, off, 0, 16); }
DEV void st16(rsrc_t r, unsigned off, v4u v) { __builtin_amdgcn_raw_buffer_store_b128(v, r, off, 0, 16); }
DEV void st4(rsrc_t r, unsigned off, unsigned v) { __builtin_amdgcn_raw_buffer_store_b32(v, r, off, 0, 16); }
DEV v4u ld16s(rsrc_t r, unsigned v, unsigned so) { return __builtin_amdgcn_raw_buffer_load_b128(r, v, so, 16); }
DEV v2u ld8s(rsrc_t r, unsigned v, unsigned so) { return __builtin_amdgcn_raw_buffer_load_b64(r, v, so, 16); }
DEV void st16s(rsrc_t r, unsigned v, unsigned so, v4u x) { __builtin_amdgcn_raw_buffer_store_b128(x, r, v, so, 16); }
DEV void st8s(rsrc_t r, unsigned v, unsigned so, v2u x) { __builtin_amdgcn_raw_buffer_store_b64(x, r, v, so, 16); }
DEV void drain() { asm volatile("s_waitcnt vmcnt(0)" ::: "memory"); }
DEV void bar() { asm volatile("s_waitcnt lgkmcnt(0)" ::: "memory"); __builtin_amdgcn_s_barrier(); asm volatile("" ::: "memory"); }
DEV float bflo(unsigned u) { return __uint_as_float(u << 16); }
DEV float bfhi(unsigned u) { return __uint_as_float(u & 0xffff0000u); }
DEV unsigned pk2(float a, float b) { bf2 v; v[0] = (__bf16)a; v[1] = (__bf16)b; return __builtin_bit_cast(unsigned, v); }
DEV float rb(float a) { return (float)(__bf16)a; }
DEV unsigned nz(unsigned u) { return max(u, 1u); }
DEV v4u nz4(v4u v) { v.x = nz(v.x); v.y = nz(v.y); v.z = nz(v.z); v.w = nz(v.w); return v; }
DEV unsigned min4(v4u v) { return min(min(v.x, v.y), min(v.z, v.w)); }
DEV unsigned rmap(unsigned L, unsigned par) { return ((L >> 13) << 14) + (L & 8191u) + par * 8192u; }
DEV v4f mfma32(v4u a, v4u b, v4f c) { return __builtin_amdgcn_mfma_f32_16x16x32_bf16(__builtin_bit_cast(bf8, a), __builtin_bit_cast(bf8, b), c, 0, 0, 0); }
DEV v4f mfma16(v2u a, v2u b, v4f c) { return __builtin_amdgcn_mfma_f32_16x16x16bf16_1k(__builtin_bit_cast(v4s, a), __builtin_bit_cast(v4s, b), c, 0, 0, 0); }
#define SB() __builtin_amdgcn_sched_barrier(0)
DEV void smf_a(v4f& c, v4u a, v8u w, int idx) { asm volatile("v_smfmac_f32_16x16x64_bf16 %0, %1, %2, %3" : "+v"(c) : "v"(a), "a"(w), "v"(idx)); }
DEV void smf_v(v4f& c, v4u a, v8u w, int idx) { asm volatile("v_smfmac_f32_16x16x64_bf16 %0, %1, %2, %3" : "+v"(c) : "v"(a), "v"(w), "v"(idx)); }
DEV void mf_av0(v4f& c, v4u a, v4u b) { asm volatile("v_mfma_f32_16x16x32_bf16 %0, %1, %2, 0" : "=&v"(c) : "a"(a), "v"(b)); }
DEV void mf_vv0(v4f& c, v4u a, v4u b) { asm volatile("v_mfma_f32_16x16x32_bf16 %0, %1, %2, 0" : "=&v"(c) : "v"(a), "v"(b)); }
DEV void mf_av(v4f& c, v4u a, v4u b) { asm volatile("v_mfma_f32_16x16x32_bf16 %0, %1, %2, %0" : "+v"(c) : "a"(a), "v"(b)); }
DEV void mf_va0(v4f& c, v4u a, v4u b) { asm volatile("v_mfma_f32_16x16x32_bf16 %0, %1, %2, 0" : "=&v"(c) : "v"(a), "a"(b)); }
DEV void mf_va(v4f& c, v4u a, v4u b) { asm volatile("v_mfma_f32_16x16x32_bf16 %0, %1, %2, %0" : "+v"(c) : "v"(a), "a"(b)); }
DEV void mf_vv(v4f& c, v4u a, v4u b) { asm volatile("v_mfma_f32_16x16x32_bf16 %0, %1, %2, %0" : "+v"(c) : "v"(a), "v"(b)); }
DEV void settle2(v4f& a, v4f& b) { asm volatile("s_nop 7" : "+v"(a), "+v"(b)); }
DEV void settle3(v4f& a, v4f& b, v4f& c) { asm volatile("s_nop 7" : "+v"(a), "+v"(b), "+v"(c)); }
DEV void late2(v4f& a, v4f& b) { asm volatile("" : "+v"(a), "+v"(b)); }
template <int CTRL> DEV float dpp(float v) { return __uint_as_float(__builtin_amdgcn_update_dpp(0u, __float_as_uint(v), CTRL, 0xf, 0xf, true)); }
#define DPPSTEP(op, ctl) "s_nop 1\n\t" op " %0, %0, %0 " ctl " row_mask:0xf bank_mask:0xf\n\t"
DEV float max8(float m) {
    asm volatile(DPPSTEP("v_max_f32_dpp", "quad_perm:[1,0,3,2]") DPPSTEP("v_max_f32_dpp", "quad_perm:[2,3,0,1]") DPPSTEP("v_max_f32_dpp", "row_half_mirror") : "+v"(m));
    return m;
}
DEV float sum8(float m) {
    asm volatile(DPPSTEP("v_add_f32_dpp", "quad_perm:[1,0,3,2]") DPPSTEP("v_add_f32_dpp", "quad_perm:[2,3,0,1]") DPPSTEP("v_add_f32_dpp", "row_half_mirror") : "+v"(m));
    return m;
}
DEV float sum16(float m) {
    asm volatile(DPPSTEP("v_add_f32_dpp", "quad_perm:[1,0,3,2]") DPPSTEP("v_add_f32_dpp", "quad_perm:[2,3,0,1]")
                 DPPSTEP("v_add_f32_dpp", "row_half_mirror") DPPSTEP("v_add_f32_dpp", "row_mirror") : "+v"(m));
    return m;
}
DEV void mf16_vv(v4f& c, v2u a, v2u b) { asm volatile("v_mfma_f32_16x16x16_bf16 %0, %1, %2, %0" : "+v"(c) : "v"(a), "v"(b)); }
DEV void late3(v4f& a, v4f& b, v4f& c) { asm volatile("" : "+v"(a), "+v"(b), "+v"(c)); }
DEV void late1(v4f& a) { asm volatile("" : "+v"(a)); }
DEV void lgkm0() { asm volatile("s_waitcnt lgkmcnt(0)" ::: "memory"); }
DEV v4u cat4(v2u a, v2u b) { return __builtin_shufflevector(a, b, 0, 1, 2, 3); }
DEV v8u cat8(v4u a, v4u b) { return __builtin_shufflevector(a, b, 0, 1, 2, 3, 4, 5, 6, 7); }
DEV v4u lo4(v8u v) { return __builtin_shufflevector(v, v, 0, 1, 2, 3); }
DEV v4u hi4(v8u v) { return __builtin_shufflevector(v, v, 4, 5, 6, 7); }
DEV v4u lds16(const char* p) { return *(const LAS v4u*)(p); }
DEV void sts16(char* p, v4u v) { *(LAS v4u*)(p) = v; }
DEV v2u trread(const char* p) { return __builtin_bit_cast(v2u, __builtin_amdgcn_ds_read_tr16_b64_v4i16((LAS v4s*)(p))); }
DEV void swap32(float& a, float& b) { auto r = __builtin_amdgcn_permlane32_swap(__float_as_uint(a), __float_as_uint(b), false, false); a = __uint_as_float(r[0]); b = __uint_as_float(r[1]); }
DEV void swap16(unsigned& a, unsigned& b) { auto r = __builtin_amdgcn_permlane16_swap(a, b, false, false); a = r[0]; b = r[1]; }

DEV float xr16max(float v) { unsigned a = __float_as_uint(v), b = a; swap16(a, b); return fmaxf(__uint_as_float(a), __uint_as_float(b)); }
DEV float xr16sum(float v) { unsigned a = __float_as_uint(v), b = a; swap16(a, b); return __uint_as_float(a) + __uint_as_float(b); }
DEV bool spin_fail(rsrc_t rs, unsigned& spins) {
    ++spins;
    if (spins > SPIN_CAP) { st4(rs, C_ABORT, 1u); return true; }
    if ((spins & 127u) == 0u) { if (__builtin_amdgcn_readfirstlane(ld4(rs, C_ABORT))) return true; }
    return false;
}
DEV bool wait_groups(rsrc_t rs, unsigned base, int n, unsigned need, int lane, unsigned& seen) {
    if (seen >= need) return true;
    const unsigned off = base + (unsigned)(lane < n ? lane : 0) * 16u;
    for (unsigned spins = 0;;) {
        unsigned m = min4(ld16(rs, off));
        asm volatile("" ::: "memory");
        if ((__all(m >= need))) {
            unsigned lo = need;
#pragma unroll
            for (int k = 4; k >= 1; k >>= 1) if (__all(m >= lo + k)) lo += k;
            seen = lo;
            return true;
        }
        if (__any(m + 1u < need)) __builtin_amdgcn_s_sleep(40); else __builtin_amdgcn_s_sleep(POLLSLEEP);
        if (spin_fail(rs, spins)) return false;
    }
}
DEV bool peek_groups(rsrc_t rs, unsigned base, int n, unsigned need, int lane, unsigned& seen) {
    if (seen >= need) return true;
    const unsigned m = min4(ld16(rs, base + (unsigned)(lane < n ? lane : 0) * 16u));
    asm volatile("" ::: "memory");
    if (!__all(m >= need)) return false;
    unsigned lo = need;
#pragma unroll
    for (int k = 4; k >= 1; k >>= 1) if (__all(m >= lo + k)) lo += k;
    seen = lo;
    return true;
}
DEV bool wait_word(rsrc_t rs, unsigned off, unsigned need, unsigned& got) {
    for (unsigned spins = 0;;) {
        got = __builtin_amdgcn_readfirstlane(ld4(rs, off));
        asm volatile("" ::: "memory");
        if ((got >= need)) return true;
        __builtin_amdgcn_s_sleep(POLLSLEEP);
        if (spin_fail(rs, spins)) return false;
    }
}
DEV bool wait_par(rsrc_t rs, unsigned word, unsigned& par) {
    for (unsigned spins = 0;;) {
        const v4u u = ld16(rs, word), w = ld16(rs, word + 16);
        asm volatile("" ::: "memory");
        if ((__all(min(min4(u), min4(w)) != 0u))) {
            par = __builtin_amdgcn_readfirstlane((min(u.y, w.y) < min(u.x, w.x) ? 1u : 0u) + (min(u.w, w.w) < min(u.z, w.z) ? 2u : 0u));
            return true;
        }
        __builtin_amdgcn_s_sleep(POLLSLEEP);
        if (spin_fail(rs, spins)) return false;
    }
}
DEV bool wait_head(rsrc_t rs, unsigned base, int h, unsigned need, int lane, unsigned& seen) {
    const int j0 = (6 * h) >> 3, j1 = (6 * h + 5) >> 3;
    return wait_groups(rs, base + j0 * 16, j1 - j0 + 1, need, lane, seen);
}
DEV void post(rsrc_t rs, unsigned group, int wave, int lane, unsigned val) {
    drain();
    if (lane == 0) st4(rs, group + wave * 4, val);
}

DEV void dma16(const void* gbase, unsigned voff, unsigned ldsaddr) {
    unsigned keep;
    asm volatile("s_mov_b32 %0, m0\n\ts_mov_b32 m0, %3\n\ts_nop 0\n\tglobal_load_lds_dwordx4 %1, %2\n\ts_mov_b32 m0, %0"
                 : "=&s"(keep) : "v"(voff), "s"(gbase), "s"(ldsaddr) : "memory");
}
DEV void vmwait_n(int n) { asm volatile("s_waitcnt vmcnt(%0)" :: "n"(n) : "memory"); }
template <int N> DEV void vmwait() { asm volatile("s_waitcnt vmcnt(%0)" :: "n"(N) : "memory"); }
template <int ROWB> DEV int stg_f(int row) { return (ROWB == 256 || ROWB == 512) ? ((row & 3) | (((row >> 3) & 1) << 2)) : (((row >> 1) & 1) | (((row >> 3) & 1) << 1)); }
template <int ROWB> DEV int stg_src(int row, int ch) { return ch ^ (stg_f<ROWB>(row) << 1); }
template <int ROWB> DEV v8u tr_frag(const char* tile, int r0, int lane, int colrun) {
    const int g = lane >> 4, q = (lane & 15) >> 2, row = 8 * g + q;
    const char* a = tile + (r0 + row) * ROWB + ((colrun * 2) ^ (stg_f<ROWB>(row) << 5));
    const v2u x0 = trread(a), x1 = trread(a + 4 * ROWB), x2 = trread(a + 32 * ROWB), x3 = trread(a + 36 * ROWB);
    v8u r; r[0] = x0.x; r[1] = x0.y; r[2] = x1.x; r[3] = x1.y; r[4] = x2.x; r[5] = x2.y; r[6] = x3.x; r[7] = x3.y;
    return r;
}
constexpr int STG = 24576;

typedef unsigned long long u64;
#define MLDO(d, v, so, m, off) "s_mov_b64 exec, " m "\n\tbuffer_load_dwordx4 " d ", " v ", %[rs], " so " offen offset:" off " sc1\n\t"
#define MLD(d, v, so, m) MLDO(d, v, so, m, "0")
DEV bool all_nz(unsigned m) { return (__all(m != 0u)); }
DEV void poll4(v4u rsv, v4u (&A)[4], unsigned v, unsigned s0, unsigned s1, const u64 (&m)[4]) {
    asm volatile("s_nop 4\n\t" MLD("%0", "%4", "%5", "%7") MLDO("%1", "%4", "%5", "%8", "1024") MLD("%2", "%4", "%6", "%9") MLDO("%3", "%4", "%6", "%10", "1024")
                 "s_mov_b64 exec, -1\n\ts_waitcnt vmcnt(0)"
                 : "+v"(A[0]), "+v"(A[1]), "+v"(A[2]), "+v"(A[3]) : "v"(v), "s"(s0), "s"(s1), "s"(m[0]), "s"(m[1]), "s"(m[2]), "s"(m[3]), [rs] "s"(rsv) : "memory");
}
DEV bool gather_mem(rsrc_t rs, v4u rsv, unsigned par0, int s, int lane, int wave, char* memf, v4u (&A)[4]) {
    const unsigned L = L_MEM + (unsigned)(s & 3) * MEM_SLOT, l16 = lane * 16;
    const unsigned o0 = __builtin_amdgcn_readfirstlane(rmap(L, par0)), o1 = __builtin_amdgcn_readfirstlane(rmap(L + 2048 + wave * 2048, par0));
    bool good = true;
    A[0] = (v4u){0, 0, 0, 0}; A[1] = A[0]; A[2] = A[0]; A[3] = A[0];
    u64 m[4] = {~0ull, ~0ull, ~0ull, ~0ull};
    for (unsigned spins = 0;;) {
        poll4(rsv, A, l16, o0, o1, m);
        unsigned q[4];
#pragma unroll
        for (int i = 0; i < 4; ++i) q[i] = min4(A[i]);
        if (all_nz(min(min(q[0], q[1]), min(q[2], q[3])))) break;
#pragma unroll
        for (int i = 0; i < 4; ++i) m[i] = __ballot(q[i] == 0u);
        if (spin_fail(rs, spins)) { good = false; break; }
    }
    sts16(memf + (2 + 2 * wave) * 1024 + l16, A[2]); sts16(memf + (3 + 2 * wave) * 1024 + l16, A[3]);
    return good;
}
DEV unsigned wm_off(int s, unsigned L, unsigned par) {
    const unsigned sl = s & 3;
    return sl < 2 ? rmap(L_WM + sl * WM_SLOT + L, par & 1u) : PAGE + rmap((sl - 2) * WM_SLOT + L, par >> 1);
}

DEV unsigned wm_base(int s, unsigned par) { return wm_off(s, 0u, par); }
constexpr int WOFF = 40960;

constexpr int S_PASS[20] = {0, 1, 2, 3, 4, 5, 6, 7, 8, 9, 10, 12, 14, 16, 18, 11, 13, 15, 17, 19};
constexpr int s_pos(int ms) { for (int i = 0; i < 20; ++i) if (S_PASS[i] == ms) return i; return -1; }

DEV v4u ws_desc(const Params& p) { const u64 a = (u64)p.ws; return (v4u){(unsigned)a, (unsigned)(a >> 32) & 0xffffu, B_END, 0x00020000u}; }
DEV void role_S(const Params& p, rsrc_t rs, int h, char* lds, int t) {
    const v4u rsv = ws_desc(p);
    const int lane = t & 63, wave = __builtin_amdgcn_readfirstlane(t >> 6), n = lane & 15, g = lane >> 4;
    char* const MEMF = lds;
    char* const QK = lds + 20480;
    char* const SPX = lds + 28160;
    char* const WA = lds + WOFF;
    constexpr int SPIN = 31;
    const int efull = (wave & 1) ? 48 * wave + 16 : 48 * wave, ehalf = (wave & 1) ? 48 * wave : 48 * wave + 32;
    v8u W[45];
    {
        unsigned vo[6];
#pragma unroll
        for (int k = 0; k < 6; ++k) {
            const int i = (wave * 6 + k) * 64 + lane, ch = stg_src<384>(i / 24, i % 24);
            vo[k] = (unsigned)((i / 24) * HD * 2 + ch * 16);
        }
        const unsigned ldsb = (unsigned)(size_t)(LAS char*)WA + wave * 6144;
        auto issue = [&](int ps) {
            const uint16_t* src = (S_PASS[ps] < 10 ? p.Wq : p.Wk) + (size_t)(S_PASS[ps] % 10) * 64 * HD + h * DH;
#pragma unroll
            for (int k = 0; k < 6; ++k) dma16(src, vo[k], ldsb + (ps % 5) * STG + k * 1024);
        };
#pragma unroll
        for (int ps = 0; ps < 4; ++ps) issue(ps);
#pragma unroll
        for (int ps = 0; ps < 20; ++ps) {
            if (ps <= 16) vmwait<18>(); else if (ps == 17) vmwait<12>(); else if (ps == 18) vmwait<6>(); else vmwait<0>();
            bar();
            if (ps + 4 < 20) issue(ps + 4);
            const char* tile = WA + (ps % 5) * STG;
            v8u f[3];
#pragma unroll
            for (int tl = 0; tl < 3; ++tl) f[tl] = tr_frag<384>(tile, 0, lane, (tl < 2 ? efull + 16 * tl : ehalf) + 4 * (lane & 3));
            if (ps < 15) {
#pragma unroll
                for (int tl = 0; tl < 3; ++tl) { W[ps * 3 + tl] = f[tl]; if (ps * 3 + tl < SPIN) asm volatile("" : "+a"(W[ps * 3 + tl])); }
            } else {
                bar();
#pragma unroll
                for (int tl = 0; tl < 3; ++tl) {
                    char* d = WA + (ps % 5) * STG + (wave * 3 + tl) * 2048 + lane * 16;
                    sts16(d, lo4(f[tl])); sts16(d + 1024, hi4(f[tl]));
                }
            }
        }
    }
    unsigned par;
    if (!wait_par(rs, C_PAR, par)) return;
    const unsigned par0 = par & 1u, fb = rmap(L_FLAGS, par0 ^ 1u);
    const int idx = (lane & 8) ? 0xEEEE : 0x4444;
    const int bfull = efull >> 5, bhalf = ehalf >> 5;
    const int gh = 2 * (wave & 1) + (g >> 1);
    const float sc2 = 0.07216878364870322f * 1.4426950408889634f;
    v4u kf[2]; v2u kh[2];
    unsigned seenK = 0;
    auto fetch_kx = [&](int s) -> bool {
        if (!wait_head(rs, C_FK, h, s + 1, lane, seenK)) return false;
        const unsigned b = B_KX + (unsigned)(s * NH + h) * 12288u;
#pragma unroll
        for (int kt = 0; kt < 2; ++kt) {
            kf[kt] = ld16s(rs, lane * 16, b + (kt * 6 + bfull) * 1024);
            kh[kt] = ld8s(rs, (gh * 16 + n) * 16 + (g & 1) * 8, b + (kt * 6 + bhalf) * 1024);
        }
        return true;
    };
    const char* const wlb = WA + wave * 6144 + lane * 16;
    auto wl = [&](int i) -> v8u { const char* d = wlb + (i / 3) * STG + (i % 3) * 2048; return cat8(lds16(d), lds16(d + 1024)); };
    constexpr int NWL = 5;
    const int q4 = n >> 2, pp = lane & 3;
    char* const wrF = QK + (efull + n) * 40 + (lane >> 5) * 16 + (g & 1) * 8;
    char* const wrH = QK + (ehalf + n) * 40 + (lane >> 5) * 16 + (g & 1) * 8;
    const char* const trF = QK + (efull + 8 * g + q4) * 40 + pp * 8;
    const char* const trH = QK + (ehalf + 4 * g + q4) * 40 + pp * 8;
    const char* const trAll = QK + (8 * g + q4) * 40 + pp * 8;
    const char* const arot = MEMF + lane * 16;
    char* const spw = SPX + wave * 1408 + (n & 7) * 176;
    const char* const sprd = SPX + (lane >> 3) * 176;
    const unsigned hoff = (h >> 3) * 8192 + (h & 7) * 640;
    const unsigned vp = !(lane & 1) ? (lane >> 3) * 64 + (lane & 6) * 8 : (lane & 7) == 1 ? 512 + (lane >> 3) * 16 : OOB;
    if (!fetch_kx(0)) return;
    bool good = true;
    for (int s = 0; s < NSEG && good; ++s) {
        v4f aq[3], ak[3];
#pragma unroll
        for (int tl = 0; tl < 3; ++tl) { aq[tl] = (v4f){0, 0, 0, 0}; ak[tl] = (v4f){0, 0, 0, 0}; }
        late3(aq[0], aq[1], aq[2]); late3(ak[0], ak[1], ak[2]);
        v8u WL[NWL];
#pragma unroll
        for (int i = 0; i < NWL; ++i) WL[i] = wl(i);
        SB();
        v4u A[10];
        auto mm = [&](v4f& acc, int ms, int tl) {
            const int ps = s_pos(ms);
            if (ps * 3 + tl < SPIN) smf_a(acc, A[ms % 10], W[ps * 3 + tl], idx);
            else if (ps < 15) smf_v(acc, A[ms % 10], W[ps * 3 + tl], idx);
            else { const int li = (ps - 15) * 3 + tl; smf_v(acc, A[ms % 10], WL[li % NWL], idx); if (li + NWL < 15) { WL[li % NWL] = wl(li + NWL); SB(); } }
        };
        {
            v4u own[4];
            good = gather_mem(rs, rsv, par0, s, lane, wave, MEMF, own);
            A[0] = own[0]; A[1] = own[1];
        }
        SB();
        mm(aq[0], 0, 0); mm(ak[0], 10, 0); mm(aq[1], 0, 1); mm(ak[1], 10, 1);
        bar();
        A[2] = lds16(arot + 2 * 1024); A[3] = lds16(arot + 3 * 1024);
        SB();
        mm(aq[2], 0, 2); mm(ak[2], 10, 2);
#pragma unroll
        for (int j = 1; j < 10; ++j) {
            if (j + 3 < 10) { A[j + 3] = lds16(arot + (j + 3) * 1024); SB(); }
#pragma unroll
            for (int tl = 0; tl < 3; ++tl) { mm(aq[tl], j, tl); mm(ak[tl], 10 + j, tl); }
        }
        asm volatile("s_nop 3" : "+v"(aq[0]), "+v"(ak[0]));
        auto comb = [&](int tl) {
            float r[4];
#pragma unroll
            for (int i = 0; i < 4; ++i) { float a = aq[tl][i], b = ak[tl][i]; swap32(a, b); r[i] = a + b; }
            *(LAS v2u*)(tl < 2 ? wrF + tl * 640 : wrH) = (v2u){pk2(r[0], r[1]), pk2(r[2], r[3])};
            SB();
        };
        comb(0);
        late2(aq[1], ak[1]); comb(1);
        {
            const v4u Ff = cat4(trread(trF), trread(trF + 160));
            SB();
            late2(aq[2], ak[2]); comb(2);
            const v2u Fh = trread(trH);
            SB();
            v4f sx0, sx1, sm;
            mf_vv0(sx0, kf[0], Ff); mf_vv0(sx1, kf[1], Ff); mf_vv0(sm, Ff, Ff);
            asm volatile("s_nop 3" ::);
            mf16_vv(sx0, kh[0], Fh); mf16_vv(sx1, kh[1], Fh); mf16_vv(sm, Fh, Fh);
            asm volatile("s_nop 5" : "+v"(sx0), "+v"(sx1), "+v"(sm));
            if (n < 8) {
                *(LAS v4f*)(spw + g * 32) = sx0; *(LAS v4f*)(spw + g * 32 + 16) = sx1;
                if (g >= 2) *(LAS v4f*)(spw + 96 + g * 16) = sm;
            }
        }
        unsigned pslot = rmap(L_P + (unsigned)(s & 3) * P_SLOT + hoff, par0);
        asm volatile("" : "+s"(pslot));
        bar();
        if (wave == 0) {
            if (s + 1 < NSEG) {
                v4f xs[4]; float ys[4];
#pragma unroll
                for (int w = 0; w < 4; ++w) { xs[w] = *(const LAS v4f*)(sprd + w * 1408 + (lane & 7) * 16); ys[w] = *(const LAS float*)(sprd + w * 1408 + 128 + (lane & 7) * 4); }
                SB();
                v4f x = (xs[0] + xs[1]) + (xs[2] + xs[3]);
                float y = (ys[0] + ys[1]) + (ys[2] + ys[3]);
                const float ms = max8(fmaxf(fmaxf(fmaxf(x[0], x[1]), fmaxf(x[2], x[3])), y)) * sc2;
#pragma unroll
                for (int i = 0; i < 4; ++i) x[i] = __builtin_amdgcn_exp2f(x[i] * sc2 - ms);
                y = __builtin_amdgcn_exp2f(y * sc2 - ms);
                const float inv = __builtin_amdgcn_rcpf(sum8(((x[0] + x[1]) + (x[2] + x[3])) + y));
                x *= inv; y *= inv;
                const float d0 = __uint_as_float(nz(pk2(x[0], x[1]))), d1 = __uint_as_float(nz(pk2(x[2], x[3])));
                const float dm = __uint_as_float(nz(pk2(dpp<0xB1>(y), y)));
                const float m1 = dpp<0x102>(dm), m2 = dpp<0x104>(dm), m3 = dpp<0x106>(dm);
                const float e0 = dpp<0xB1>(d0), e1 = dpp<0xB1>(d1);
                const bool odd = lane & 1;
                const v4u o = {__float_as_uint(odd ? dm : d0), __float_as_uint(odd ? m1 : d1), __float_as_uint(odd ? m2 : e0), __float_as_uint(odd ? m3 : e1)};
                st16s(rs, vp, pslot, o);
            }
            if (s > 0 && lane < 40) st16s(rs, lane * 16, rmap(L_P + (unsigned)((s - 1) & 3) * P_SLOT + hoff, par0), (v4u){0, 0, 0, 0});
        } else if (wave == 3) {
#pragma unroll
            for (int b = 0; b < 6; ++b) {
                const v4u F = cat4(trread(trAll + b * 1280), trread(trAll + b * 1280 + 160));
                if (n >= 8) st16s(rs, (g * 8 + n - 8) * 16, B_KM + (unsigned)(s * NH + h) * 3072u + b * 512, F);
            }
            post(rs, C_FKM + h * 128, 0, lane, s + 1);
        }
        if (s + 1 < NSEG) { if (!fetch_kx(s + 1)) return; }
    }
}

struct VPass { int kind, id, buf, res; };
constexpr int VR0 = WOFF, VR1 = WOFF + 24576, VR2 = WOFF + 49152, VR3 = WOFF + 73728, VR4 = VR3 + 16384, VR5 = VR4 + 16384, VE0 = 0, VE1 = 16384;
constexpr VPass V_PASS[25] = {
    {0, 0, VR0, 0}, {0, 1, VR1, 0}, {0, 2, VR2, 0}, {0, 4, VR3, 0}, {0, 5, VR0, 0}, {0, 7, VR1, 0}, {0, 8, VR2, 0},
    {1, 0, VR5, 0}, {1, 1, VE0, 0}, {1, 2, VE1, 0}, {0, 3, VR0, 1}, {0, 6, VR1, 1}, {0, 9, VR2, 1},
    {1, 3, VR3, 0}, {1, 4, VR4, 0}, {1, 5, VR5, 0}, {1, 6, VE0, 0}, {1, 7, VE1, 0}, {1, 8, VR3, 0}, {1, 9, VR4, 0}, {1, 10, VR5, 0}, {1, 11, VE0, 0},
    {1, 12, VR3, 1}, {1, 13, VR4, 1}, {1, 14, VR5, 1}};
constexpr bool v_overlap(int a, int b) {
    const int a0 = V_PASS[a].buf, a1 = a0 + (V_PASS[a].kind ? 16384 : 24576), b0 = V_PASS[b].buf, b1 = b0 + (V_PASS[b].kind ? 16384 : 24576);
    return a0 < b1 && b0 < a1;
}
struct VTab { int issued[26], wait[25]; };
constexpr VTab v_tab() {
    VTab tb{};
    int j = 0;
    for (int cur = -1; cur < 25; ++cur) {
        for (; j < 25 && j <= cur + 6; ++j) {
            bool ok = true;
            for (int k = cur < 0 ? 0 : cur; k < j; ++k) if (v_overlap(k, j)) ok = false;
            if (!ok) break;
        }
        tb.issued[cur + 1] = j;
    }
    for (int i = 0; i < 25; ++i) { int c = 0; for (int k = i + 1; k < tb.issued[i]; ++k) c += V_PASS[k].kind ? 4 : 6; tb.wait[i] = c; }
    return tb;
}
constexpr VTab V_TAB = v_tab();
static_assert(V_TAB.issued[25] == 25, "load schedule incomplete");
constexpr int v_reg(int kind, int id, int j) {
    if (kind == 0) {
        if (id && id % 3 == 0) return -1 - ((id / 3 - 1) * 3 + j);
        return (id - id / 3) * 3 + j;
    }
    if (id >= 12) return -1 - (9 + (id - 12) * 2 + j);
    return 21 + id * 2 + j;
}
constexpr int v_lds(int i) {
    if (i < 9) return (i / 3 == 0 ? VR0 : i / 3 == 1 ? VR1 : VR2) + (i % 3) * 2048;
    return ((i - 9) / 2 == 0 ? VR3 : (i - 9) / 2 == 1 ? VR4 : VR5) + ((i - 9) & 1) * 2048;
}

DEV void role_V(const Params& p, rsrc_t rs, int h, char* lds, int t) {
    const v4u rsv = ws_desc(p);
    const int lane = t & 63, wave = __builtin_amdgcn_readfirstlane(t >> 6), n = lane & 15, g = lane >> 4;
    char* const MEMF = lds;
    char* const VI = lds + 20480;
    constexpr int VPIN = 31;
    v8u W[45];
    {
        unsigned vo[6], vo2[4];
#pragma unroll
        for (int k = 0; k < 6; ++k) {
            const int i = (wave * 6 + k) * 64 + lane, ch = stg_src<384>(i / 24, i % 24);
            vo[k] = (unsigned)((i / 24) * HD * 2 + ch * 16);
        }
#pragma unroll
        for (int k = 0; k < 4; ++k) { const int i = (wave * 4 + k) * 64 + lane, r = i >> 4, ch = stg_src<256>(r, i & 15); vo2[k] = (unsigned)(r * D * 2 + (ch >> 2) * 320 + (ch & 3) * 16); }
        const unsigned ldsl = (unsigned)(size_t)(LAS char*)lds;
        auto issue = [&](auto ic) {
            constexpr VPass P = V_PASS[decltype(ic)::value];
            if constexpr (P.kind == 0) {
                const uint16_t* src = p.Wv + (size_t)P.id * 64 * HD + h * DH;
#pragma unroll
                for (int k = 0; k < 6; ++k) dma16(src, vo[k], ldsl + P.buf + wave * 6144 + k * 1024);
            } else {
                const uint16_t* src = p.Wo + ((size_t)h * DH + (P.id / 5) * 64) * D + (P.id % 5) * 32;
#pragma unroll
                for (int k = 0; k < 4; ++k) dma16(src, vo2[k], ldsl + P.buf + wave * 4096 + k * 1024);
            }
        };
        sfor<0, V_TAB.issued[0]>(issue);
        sfor<0, 25>([&](auto ic) {
            constexpr int ps = decltype(ic)::value;
            constexpr VPass P = V_PASS[ps];
            constexpr int nf = P.kind ? 2 : 3;
            vmwait<V_TAB.wait[ps]>();
            bar();
            sfor<V_TAB.issued[ps], V_TAB.issued[ps + 1]>(issue);
            const char* tile = lds + P.buf;
            v8u f[nf];
#pragma unroll
            for (int j = 0; j < nf; ++j)
                f[j] = P.kind ? tr_frag<256>(tile, 0, lane, 32 * wave + 16 * j + 4 * (lane & 3)) : tr_frag<384>(tile, 0, lane, 48 * wave + 16 * j + 4 * (lane & 3));
            if constexpr (!P.res) {
#pragma unroll
                for (int j = 0; j < nf; ++j) { const int ri = v_reg(P.kind, P.id, j); W[ri] = f[j]; if (ri < VPIN) asm volatile("" : "+a"(W[ri])); }
            } else {
                bar();
#pragma unroll
                for (int j = 0; j < nf; ++j) { char* d = lds + P.buf + (wave * nf + j) * 2048 + lane * 16; sts16(d, lo4(f[j])); sts16(d + 1024, hi4(f[j])); }
            }
        });
        bar();
    }
    unsigned par;
    if (!wait_par(rs, C_PAR, par)) return;
    const unsigned par0 = par & 1u;
    const int idx = (lane & 8) ? 0xEEEE : 0x4444;
    const char* const gb3 = lds + wave * 6144 + lane * 16;
    const char* const gb2 = lds + wave * 4096 + lane * 16;
    auto gl = [&](int i) -> v8u { const char* d = (i < 9 ? gb3 : gb2) + v_lds(i); return cat8(lds16(d), lds16(d + 1024)); };
    const int q4 = n >> 2, pp = lane & 3;
    char* const wrAB = VI + (48 * wave + (lane < 32 ? 0 : 16) + n) * 16 + (g & 1) * 8;
    char* const wrC = VI + (48 * wave + 32 + n) * 16 + (g & 1) * 8 + (g >> 1) * 4;
    const char* const trV = VI + (16 * g + 4 * (q4 >> 1) + (q4 & 1) + 2 * (pp >> 1)) * 16 + (pp & 1) * 8;
    const char* const arot = MEMF + lane * 16;
    const unsigned vw4 = (2 * (g & 1) + (g >> 1)) * 16384 + n * 16, vw2 = (g >> 1) * 16384 + n * 16;
    bool good = true;
    for (int s = 0; s < NSEG && good; ++s) {
        v4f av[3], ac[5][2];
#pragma unroll
        for (int tl = 0; tl < 3; ++tl) av[tl] = (v4f){0, 0, 0, 0};
        late3(av[0], av[1], av[2]);
#pragma unroll
        for (int i = 0; i < 5; ++i) { ac[i][0] = (v4f){0, 0, 0, 0}; ac[i][1] = (v4f){0, 0, 0, 0}; late2(ac[i][0], ac[i][1]); }
        v8u G[3];
#pragma unroll
        for (int i = 0; i < 3; ++i) G[i] = gl(i);
        SB();
        const unsigned wmb = wm_base(s, par) + (unsigned)(wave * 10) * 16384u + h * 256;
        auto mm = [&](v4f& acc, v4u a, int kind, int id, int j) {
            const int ri = v_reg(kind, id, j);
            if (ri >= VPIN) smf_v(acc, a, W[ri], idx);
            else if (ri >= 0) smf_a(acc, a, W[ri], idx);
            else { const int li = -1 - ri; smf_v(acc, a, G[li % 3], idx); if (li + 3 < 15) { G[li % 3] = gl(li + 3); SB(); } }
        };
        v4u A[10];
        {
            v4u own[4];
            good = gather_mem(rs, rsv, par0, s, lane, wave, MEMF, own);
            A[0] = own[0]; A[1] = own[1];
        }
        bar();
        A[2] = lds16(arot + 2 * 1024); A[3] = lds16(arot + 3 * 1024); A[4] = lds16(arot + 4 * 1024);
        SB();
#pragma unroll
        for (int j = 0; j < 10; ++j) {
            if (j >= 1 && j + 4 < 10) { A[j + 4] = lds16(arot + (j + 4) * 1024); SB(); }
#pragma unroll
            for (int tl = 0; tl < 3; ++tl) mm(av[tl], A[j], 0, j, tl);
        }
        float r[4];
        asm volatile("s_nop 6" : "+v"(av[0]), "+v"(av[1]));
#pragma unroll
        for (int i = 0; i < 4; ++i) { float a = av[0][i], b = av[1][i]; swap32(a, b); r[i] = a + b; }
        *(LAS v2u*)wrAB = (v2u){pk2(r[0], r[1]), pk2(r[2], r[3])};
        late1(av[2]);
        { float a = av[2][0], b = av[2][2]; swap32(a, b); r[0] = a + b; }
        { float a = av[2][1], b = av[2][3]; swap32(a, b); r[1] = a + b; *(LAS unsigned*)wrC = pk2(r[0], r[1]); }
        bar();
        v4u VA[3];
#pragma unroll
        for (int ks = 0; ks < 3; ++ks) VA[ks] = cat4(trread(trV + ks * 1024), trread(trV + ks * 1024 + 128));
        unsigned pkd[5][2];
        SB();
        auto half = [&](int pr, int lo) {
            v4f& a0 = ac[pr][0]; v4f& a1 = ac[pr][1];
            float c0, c1;
            { float a = a0[lo], b = a1[lo]; swap32(a, b); c0 = a + b; }
            { float a = a0[lo + 1], b = a1[lo + 1]; swap32(a, b); c1 = a + b; }
            pkd[pr][lo >> 1] = nz(pk2(c0, c1));
            SB();
        };
        auto publish = [&](int q) {
            unsigned x0 = pkd[2 * q][0], x1 = pkd[2 * q][1], y0 = pkd[q < 2 ? 2 * q + 1 : 4][0], y1 = pkd[q < 2 ? 2 * q + 1 : 4][1];
            swap16(x0, y0); swap16(x1, y1);
            if (q < 2 || !(g & 1)) st16s(rs, q < 2 ? vw4 : vw2, wmb + (unsigned)(4 * q) * 16384u, (v4u){x0, x1, y0, y1});
            SB();
        };
#pragma unroll
        for (int pr = 0; pr < 5; ++pr) {
            v4f& a0 = ac[pr][0]; v4f& a1 = ac[pr][1];
            mm(a0, VA[0], 1, pr, 0); mm(a1, VA[0], 1, pr, 1);
            mm(a0, VA[1], 1, 5 + pr, 0); mm(a1, VA[1], 1, 5 + pr, 1);
            mm(a0, VA[2], 1, 10 + pr, 0); mm(a1, VA[2], 1, 10 + pr, 1);
            if (pr > 0) { asm volatile("s_nop 1" : "+v"(ac[pr - 1][0]), "+v"(ac[pr - 1][1])); half(pr - 1, 0); half(pr - 1, 2); }
            if (pr == 2) publish(0);
            if (pr == 4) publish(1);
        }
        late2(ac[4][0], ac[4][1]);
        half(4, 0); half(4, 2);
        publish(2);
        if (QV) __builtin_amdgcn_s_sleep(QV);
        if (s > 0) {
            const unsigned zb = wm_base(s - 1, par) + (unsigned)(wave * 10) * 16384u + h * 256;
#pragma unroll
            for (int q = 0; q < 3; ++q)
                if (q < 2 || !(g & 1)) st16s(rs, q < 2 ? vw4 : vw2, zb + (unsigned)(4 * q) * 16384u, (v4u){0, 0, 0, 0});
        }
    }
}

DEV void role_R(const Params& p, rsrc_t rs, int c_, char* lds, int t) {
    const int c = __builtin_amdgcn_readfirstlane(c_);
    const v4u rsv = ws_desc(p);
    const int lane = t & 63, wave = __builtin_amdgcn_readfirstlane(t >> 6), n = lane & 15, g = lane >> 4;
    char* const RPB = lds;
    unsigned par;
    if (!wait_par(rs, C_PAR, par)) return;
    const unsigned par0 = par & 1u, fb = rmap(L_FLAGS, par0 ^ 1u);
    if (t < 16) {
        const int r = t & 7, hf = t >> 3;
        const unsigned* src = (const unsigned*)(p.mem0 + r * D + 16 * c);
        const v4u o = {src[hf], src[hf + 2], src[hf + 4], src[hf + 6]};
        st16(rs, rmap(L_MEM + c * 256 + t * 16, par0), nz4(o));
    }
    const unsigned vpub = (lane < 32 && n < 8) ? (g * 8 + n) * 16 : OOB;
    const unsigned cntaddr = (unsigned)(size_t)(LAS char*)(RPB + 8192);
    if (t == 0) *(LAS unsigned*)(RPB + 8192) = 0;
    bar();
    const unsigned vex = (n >> 3) * 640 + (n & 7) * 64 + g * 16, vpm = (4 * (n >> 3) + g) * 640 + 512 + (n & 7) * 16;
    unsigned seenU = 0;
    bool good = true;
    for (int s = 0; s < NSEG && good; ++s) {
        v4u z[8];
        if (s + 1 < NSEG) {
            if (!wait_groups(rs, C_FU + wave * 128, 8, s + 1, lane, seenU)) return;
#pragma unroll
            for (int i = 0; i < 8; ++i) z[i] = ld16s(rs, lane * 16, B_ZX + (unsigned)((s * 40 + c) * NH + wave * 8 + i) * 1024u);
        }
        const unsigned op = rmap(L_P + (unsigned)(s & 3) * P_SLOT + wave * 8192, par0), ow = wm_base(s, par) + c * 16384 + wave * 2048;
        const unsigned mnext = __builtin_amdgcn_readfirstlane(rmap(L_MEM + (unsigned)((s + 1) & 3) * MEM_SLOT + c * 256, par0));
        const unsigned mprev = __builtin_amdgcn_readfirstlane(rmap(L_MEM + (unsigned)(s & 3) * MEM_SLOT + c * 256, par0));
        v4u w0, w1;
        if (s + 1 < NSEG) {
            v4u px[4], pm;
            w0 = (v4u){0, 0, 0, 0}; w1 = w0; pm = w0; px[0] = w0; px[1] = w0; px[2] = w0; px[3] = w0;
            u64 mw0 = ~0ull, mw1 = ~0ull, mm = ~0ull, m0 = ~0ull, m1 = ~0ull, m2 = ~0ull, m3 = ~0ull;
            const unsigned l16 = lane * 16, ops = __builtin_amdgcn_readfirstlane(op), ows = __builtin_amdgcn_readfirstlane(ow);
            for (unsigned spins = 0;;) {
                asm volatile("s_nop 4\n\t" MLD("%0", "%7", "%10", "%12") MLDO("%1", "%7", "%10", "%13", "1024") MLD("%2", "%8", "%11", "%14") MLDO("%3", "%8", "%11", "%15", "1280")
                             MLDO("%4", "%8", "%11", "%16", "2560") MLDO("%5", "%8", "%11", "%17", "3840") MLD("%6", "%9", "%11", "%18") "s_mov_b64 exec, -1\n\ts_waitcnt vmcnt(0)"
                             : "+v"(w0), "+v"(w1), "+v"(px[0]), "+v"(px[1]), "+v"(px[2]), "+v"(px[3]), "+v"(pm)
                             : "v"(l16), "v"(vex), "v"(vpm), "s"(ows), "s"(ops),
                               "s"(mw0), "s"(mw1), "s"(m0), "s"(m1), "s"(m2), "s"(m3), "s"(mm), [rs] "s"(rsv) : "memory");
                if (spins == 0 && s > 0 && lane == 0) st4(rs, C_FW + c * 16 + wave * 4, s);
                const unsigned q0 = min4(w0), q1 = min4(w1), q2 = min4(px[0]), q3 = min4(px[1]), q4 = min4(px[2]), q5 = min4(px[3]), q6 = min4(pm);
                if (all_nz(min(min(min(q0, q1), min(q2, q3)), min(min(q4, q5), q6)))) break;
                mw0 = __ballot(q0 == 0u); mw1 = __ballot(q1 == 0u); m0 = __ballot(q2 == 0u); m1 = __ballot(q3 == 0u); m2 = __ballot(q4 == 0u); m3 = __ballot(q5 == 0u); mm = __ballot(q6 == 0u);
                if (spin_fail(rs, spins)) { good = false; break; }
            }
            v4f aA, aB;
            mf_vv0(aA, w0, pm); mf_vv0(aB, w1, pm);
#pragma unroll
            for (int i = 0; i < 4; ++i) { mf_vv(aA, z[2 * i], px[i]); mf_vv(aB, z[2 * i + 1], px[i]); }
            settle2(aA, aB);
            char* RP = RPB + (s & 1) * 4096;
            v4f o;
#pragma unroll
            for (int i = 0; i < 4; ++i) o[i] = aA[i] + dpp<0x128>(aB[i]);
            *(LAS v4f*)(RP + wave * 1024 + lane * 16) = o;
            unsigned arr; v4f p0, p1, p2, p3;
            asm volatile("s_mov_b64 exec, 1\n\tds_add_rtn_u32 %0, %5, %6\n\ts_mov_b64 exec, -1\n\t"
                         "ds_read_b128 %1, %7\n\tds_read_b128 %2, %7 offset:1024\n\tds_read_b128 %3, %7 offset:2048\n\tds_read_b128 %4, %7 offset:3072\n\ts_waitcnt lgkmcnt(0)"
                         : "=&v"(arr), "=&v"(p0), "=&v"(p1), "=&v"(p2), "=&v"(p3)
                         : "v"(cntaddr), "v"(1u), "v"((unsigned)(size_t)(LAS char*)(RP + lane * 16)) : "memory");
            SB();
            if ((__builtin_amdgcn_readfirstlane(arr) & 3u) == 3u) {
                const v4f sum = (p0 + p1) + (p2 + p3);
                unsigned a0 = nz(pk2(sum[0], sum[1])), b0 = nz(pk2(sum[2], sum[3]));
                swap16(a0, b0);
                float f0 = __uint_as_float(a0), f2 = f0, f1 = __uint_as_float(b0), f3 = f1;
                swap32(f0, f2); swap32(f1, f3);
                const v4u ov = {__float_as_uint(f0), __float_as_uint(f1), __float_as_uint(f2), __float_as_uint(f3)};
                st16s(rs, vpub, mnext, ov);
                if (lane < 16) st16s(rs, lane * 16, mprev, (v4u){0, 0, 0, 0});
            }
        } else {
            for (unsigned spins = 0;;) {
                w0 = ld16s(rs, lane * 16, ow); w1 = ld16s(rs, lane * 16 + 1024, ow);
                asm volatile("" ::: "memory");
                if ((__all(min(min4(w0), min4(w1)) != 0u))) break;
                if (spin_fail(rs, spins)) return;
            }
        }
        if (QR && s + 2 < NSEG) __builtin_amdgcn_s_sleep(QR);
        st16s(rs, lane * 16, B_ZM + (unsigned)(s * 40 + c) * 8192u + wave * 2048, w0);
        st16s(rs, lane * 16 + 1024, B_ZM + (unsigned)(s * 40 + c) * 8192u + wave * 2048, w1);
        if (s + 2 >= NSEG) post(rs, C_FW + c * 16, wave, lane, s + 1);
    }
}

DEV void role_U(const Params& p, rsrc_t rs, int h, char* lds, int t) {
    const int lane = t & 63, wave = __builtin_amdgcn_readfirstlane(t >> 6);
    v4u W[60];
    {
        unsigned vo2[4];
#pragma unroll
        for (int k = 0; k < 4; ++k) { const int i = (wave * 4 + k) * 64 + lane, r = i >> 4, ch = stg_src<256>(r, i & 15); vo2[k] = (unsigned)(r * D * 2 + (ch >> 2) * 320 + (ch & 3) * 16); }
        const unsigned ldsl = (unsigned)(size_t)(LAS char*)lds + wave * 4096;
        auto issue = [&](int q) {
            const uint16_t* src = p.Wo + ((size_t)h * DH + (q / 5) * 64) * D + (q % 5) * 32;
#pragma unroll
            for (int k = 0; k < 4; ++k) dma16(src, vo2[k], ldsl + (q % 8) * 16384 + k * 1024);
        };
#pragma unroll
        for (int q = 0; q < 7; ++q) issue(q);
#pragma unroll
        for (int q = 0; q < 15; ++q) {
            vmwait_n(4 * (q + 6 < 15 ? 6 : 14 - q));
            bar();
            if (q + 7 < 15) issue(q + 7);
#pragma unroll
            for (int j = 0; j < 2; ++j) {
                const v8u f = tr_frag<256>(lds + (q % 8) * 16384, 0, lane, 32 * wave + 16 * j + 4 * (lane & 3));
                const int wi = ((q % 5) * 2 + j) * 6 + (q / 5) * 2;
                W[wi] = lo4(f); W[wi + 1] = hi4(f);
                asm volatile("" : "+a"(W[wi]), "+a"(W[wi + 1]));
            }
        }
    }
    unsigned par;
    if (!wait_par(rs, C_PAR, par)) return;
    const unsigned fb = rmap(L_FLAGS, (par & 1u) ^ 1u);
    v4u nx[12];
    unsigned seenX = 0;
    auto fetch = [&](int s) -> bool {
        if (!wait_head(rs, C_FX, h, s + 1, lane, seenX)) return false;
#pragma unroll
        for (int k = 0; k < 12; ++k) nx[k] = ld16s(rs, lane * 16, B_VX + (unsigned)(s * NH + h) * 12288u + k * 1024);
        return true;
    };
    if (!fetch(0)) return;
    for (int s = 0; s < NSEG; ++s) {
        v4u vx[12];
#pragma unroll
        for (int k = 0; k < 12; ++k) { vx[k] = nx[k]; asm volatile("" : "+v"(vx[k])); }
        const bool pre = s + 1 < NSEG && seenX >= (unsigned)(s + 2);
        if (pre) { if (!fetch(s + 1)) return; }
        v4f ac[2][2];
        SB();
#pragma unroll
        for (int tl = 0; tl <= 10; ++tl) {
            if (tl < 10) {
                v4f& a0 = ac[tl & 1][0]; v4f& a1 = ac[tl & 1][1];
                mf_va0(a0, vx[0], W[tl * 6]); mf_va0(a1, vx[6], W[tl * 6]);
#pragma unroll
                for (int k = 1; k < 6; ++k) { mf_va(a0, vx[k], W[tl * 6 + k]); mf_va(a1, vx[6 + k], W[tl * 6 + k]); }
            }
            if (tl > 0) {
                v4f& a0 = ac[(tl - 1) & 1][0]; v4f& a1 = ac[(tl - 1) & 1][1];
                if (tl == 10) settle2(a0, a1); else late2(a0, a1);
                const v4u o = {pk2(a0[0], a0[1]), pk2(a0[2], a0[3]), pk2(a1[0], a1[1]), pk2(a1[2], a1[3])};
                st16s(rs, lane * 16, B_ZX + (unsigned)((s * 40 + wave * 10 + tl - 1) * NH + h) * 1024u, o);
                SB();
            }
        }
        post(rs, C_FU + h * 16, wave, lane, s + 1);
        if (!pre && s + 1 < NSEG) { if (!fetch(s + 1)) return; }
    }
}

template <int LD> DEV void load_proj_weights(const uint16_t* wbase, char* lds, int lane, int wave, v4u (&W)[60]) {
    unsigned vo[6];
#pragma unroll
    for (int k = 0; k < 6; ++k) { const int i = (wave * 6 + k) * 64 + lane; vo[k] = (unsigned)((i / 24) * LD * 2 + stg_src<384>(i / 24, i % 24) * 16); }
    const unsigned ldsb = (unsigned)(size_t)(LAS char*)lds + wave * 6144;
    auto issue = [&](int ps) {
        const uint16_t* src = wbase + (size_t)ps * 64 * LD;
#pragma unroll
        for (int k = 0; k < 6; ++k) dma16(src, vo[k], ldsb + (ps % 5) * STG + k * 1024);
    };
    const int pp = lane & 3;
    const int efull = (wave & 1) ? 48 * wave + 16 : 48 * wave, ehalf = (wave & 1) ? 48 * wave : 48 * wave + 32;
#pragma unroll
    for (int ps = 0; ps < 4; ++ps) issue(ps);
#pragma unroll
    for (int ps = 0; ps < 10; ++ps) {
        if (ps <= 6) vmwait<18>(); else if (ps == 7) vmwait<12>(); else if (ps == 8) vmwait<6>(); else vmwait<0>();
        bar();
        if (ps + 4 < 10) issue(ps + 4);
        const char* tile = lds + (ps % 5) * STG;
#pragma unroll
        for (int tl = 0; tl < 3; ++tl) {
            const v8u f = tr_frag<384>(tile, 0, lane, tl == 0 ? efull + 8 * pp : tl == 1 ? efull + 8 * pp + 4 : ehalf + 4 * pp);
            W[ps * 6 + tl] = lo4(f); W[ps * 6 + 3 + tl] = hi4(f);
            asm volatile("" : "+a"(W[ps * 6 + tl]), "+a"(W[ps * 6 + 3 + tl]));
        }
    }
    bar();
}
DEV void proj_mfma(const char* AF, int lane, const v4u (&W)[60], v4f (&acc)[3][2]) {
    v4u b[3][2];
    const char* const a0 = AF + (lane ^ (lane >> 4)) * 16;
    const char* const a1 = AF + (lane ^ (lane >> 4) ^ 4) * 16;
#pragma unroll
    for (int kk = 0; kk < 2; ++kk) { b[kk][0] = lds16((kk & 1 ? a1 : a0) + kk * 1024); b[kk][1] = lds16((kk & 1 ? a1 : a0) + (20 + kk) * 1024); }
#pragma unroll
    for (int kk = 0; kk < 20; ++kk) {
        if (kk + 2 < 20) { b[(kk + 2) % 3][0] = lds16((kk & 1 ? a1 : a0) + (kk + 2) * 1024); b[(kk + 2) % 3][1] = lds16((kk & 1 ? a1 : a0) + (22 + kk) * 1024); }
        SB();
#pragma unroll
        for (int tl = 0; tl < 3; ++tl) {
            if (kk == 0) { mf_av0(acc[tl][0], W[tl], b[0][0]); mf_av0(acc[tl][1], W[tl], b[0][1]); }
            else { mf_av(acc[tl][0], W[kk * 3 + tl], b[kk % 3][0]); mf_av(acc[tl][1], W[kk * 3 + tl], b[kk % 3][1]); }
        }
        SB();
    }
    settle3(acc[0][0], acc[1][0], acc[2][0]); settle3(acc[0][1], acc[1][1], acc[2][1]);
}
DEV void proj_store(rsrc_t rs, unsigned base, int lane, int wave, const v4f (&acc)[3][2]) {
    const int n = lane & 15, g = lane >> 4;
    const int bfull = (wave & 1) ? (3 * wave + 1) / 2 : (3 * wave) / 2, bhalf = (wave & 1) ? (3 * wave - 1) / 2 : (3 * wave) / 2 + 1;
#pragma unroll
    for (int rt = 0; rt < 2; ++rt) {
        const v4u o = {pk2(acc[0][rt][0], acc[0][rt][1]), pk2(acc[0][rt][2], acc[0][rt][3]), pk2(acc[1][rt][0], acc[1][rt][1]), pk2(acc[1][rt][2], acc[1][rt][3])};
        st16(rs, base + (rt * 6 + bfull) * 1024 + lane * 16, o);
    }
    unsigned x0 = pk2(acc[2][0][0], acc[2][0][1]), x1 = pk2(acc[2][0][2], acc[2][0][3]), y0 = pk2(acc[2][1][0], acc[2][1][1]), y1 = pk2(acc[2][1][2], acc[2][1][3]);
    swap16(x0, y0); swap16(x1, y1);
    const v4u o = {x0, x1, y0, y1};
    st16(rs, base + ((g & 1) * 6 + bhalf) * 1024 + ((2 * (wave & 1) + (g >> 1)) * 16 + n) * 16, o);
}
DEV bool fetch_hf(rsrc_t rs, int s, int t, unsigned long long& hmask, v4u (&pf)[10]) {
    for (unsigned spins = 0; !((hmask >> s) & 1ull);) {
        const unsigned f0 = ld4(rs, C_FH + (t & 63) * 4), f1 = ld4(rs, C_FH + 256 + (t & 63) * 4);
        hmask = __ballot(min(f0, f1) != 0u);
        asm volatile("" ::: "memory");
        if (((hmask >> s) & 1ull)) break;
        __builtin_amdgcn_s_sleep(POLLSLEEP);
        if (spin_fail(rs, spins)) return false;
    }
#pragma unroll
    for (int k = 0; k < 10; ++k) pf[k] = ld16s(rs, t * 16, B_HF + (unsigned)s * 40960u + k * 4096);
    return true;
}
DEV void put_hf(char* AF, int t, const v4u (&pf)[10]) {
#pragma unroll
    for (int k = 0; k < 10; ++k) sts16(AF + (k * 256 + t) * 16, pf[k]);
}

DEV void role_KX(const Params& p, rsrc_t rs, int j, char* lds, int t) {
    const int lane = t & 63, wave = __builtin_amdgcn_readfirstlane(t >> 6);
    const int jj = j < 24 ? j : j - 24;
    v4u W[80];
    {
        const uint16_t* wbase = (j < 24 ? p.Wk : p.Wv) + jj * 256;
        unsigned vo[8];
#pragma unroll
        for (int k = 0; k < 8; ++k) { const int i = (wave * 8 + k) * 64 + lane; vo[k] = (unsigned)((i >> 5) * HD * 2 + stg_src<512>(i >> 5, i & 31) * 16); }
        const unsigned ldsb = (unsigned)(size_t)(LAS char*)lds + wave * 8192;
        auto issue = [&](int ps) {
            const uint16_t* src = wbase + (size_t)ps * 64 * HD;
#pragma unroll
            for (int k = 0; k < 8; ++k) dma16(src, vo[k], ldsb + (ps % 5) * 32768 + k * 1024);
        };
#pragma unroll
        for (int ps = 0; ps < 4; ++ps) issue(ps);
#pragma unroll
        for (int ps = 0; ps < 10; ++ps) {
            if (ps <= 6) vmwait<24>(); else if (ps == 7) vmwait<16>(); else if (ps == 8) vmwait<8>(); else vmwait<0>();
            bar();
            if (ps + 4 < 10) issue(ps + 4);
#pragma unroll
            for (int tl = 0; tl < 4; ++tl) {
                const v8u f = tr_frag<512>(lds + (ps % 5) * 32768, 0, lane, 64 * wave + 32 * (tl >> 1) + 8 * (lane & 3) + 4 * (tl & 1));
                W[ps * 8 + tl] = lo4(f); W[ps * 8 + 4 + tl] = hi4(f);
                if (ps < 8) asm volatile("" : "+a"(W[ps * 8 + tl]), "+a"(W[ps * 8 + 4 + tl]));
            }
        }
        bar();
    }
    unsigned fb = 0;
    const int b0 = 8 * jj + 2 * wave, b1 = b0 + 1;
    const unsigned ob0 = (j < 24 ? B_KX : B_VX) + (b0 / 6) * 12288 + (b0 % 6) * 1024, ob1 = (j < 24 ? B_KX : B_VX) + (b1 / 6) * 12288 + (b1 % 6) * 1024;
    v4u pf[10];
    unsigned long long hmask = 0;
    if (!fetch_hf(rs, 0, t, hmask, pf)) return;
    put_hf(lds, t, pf);
    bar();
    for (int s = 0; s < NSEG; ++s) {
        if (s + 1 < NSEG) { if (!fetch_hf(rs, s + 1, t, hmask, pf)) return; }
        v4f acc[4][2];
        {
            const char* AF = lds + (s & 1) * 40960;
            const char* const a0 = AF + (lane ^ (lane >> 4)) * 16;
            const char* const a1 = AF + (lane ^ (lane >> 4) ^ 4) * 16;
            v4u b[3][2];
#pragma unroll
            for (int kk = 0; kk < 2; ++kk) { b[kk][0] = lds16((kk & 1 ? a1 : a0) + kk * 1024); b[kk][1] = lds16((kk & 1 ? a1 : a0) + (20 + kk) * 1024); }
#pragma unroll
            for (int kk = 0; kk < 20; ++kk) {
                if (kk + 2 < 20) { b[(kk + 2) % 3][0] = lds16((kk & 1 ? a1 : a0) + (kk + 2) * 1024); b[(kk + 2) % 3][1] = lds16((kk & 1 ? a1 : a0) + (22 + kk) * 1024); }
                SB();
#pragma unroll
                for (int tl = 0; tl < 4; ++tl)
#pragma unroll
                    for (int rt = 0; rt < 2; ++rt) {
                        if (kk == 0) mf_av0(acc[tl][rt], W[tl], b[0][rt]);
                        else if (kk < 16) mf_av(acc[tl][rt], W[kk * 4 + tl], b[kk % 3][rt]);
                        else mf_vv(acc[tl][rt], W[kk * 4 + tl], b[kk % 3][rt]);
                    }
                SB();
            }
            settle2(acc[3][0], acc[3][1]); late3(acc[0][0], acc[1][0], acc[2][0]); late3(acc[0][1], acc[1][1], acc[2][1]);
        }
        if (s + 1 < NSEG) put_hf(lds + ((s + 1) & 1) * 40960, t, pf);

#pragma unroll
        for (int bb = 0; bb < 2; ++bb)
#pragma unroll
            for (int rt = 0; rt < 2; ++rt) {
                const v4f x = acc[2 * bb][rt], y = acc[2 * bb + 1][rt];
                st16s(rs, lane * 16, (bb ? ob1 : ob0) + (unsigned)s * (NH * 12288) + rt * 6144, (v4u){pk2(x[0], x[1]), pk2(x[2], x[3]), pk2(y[0], y[1]), pk2(y[2], y[3])});
            }
        if (s == 0) {
            unsigned par;
            if (!wait_par(rs, C_PAR, par)) return;
            fb = rmap(L_FLAGS, (par & 1u) ^ 1u);
        }
        post(rs, (j < 24 ? C_FK : C_FX) + jj * 16, wave, lane, s + 1);
        bar();
    }
}

DEV void role_O(const Params& p, rsrc_t rs, char* lds, int t) {
    const int lane = t & 63, wave = __builtin_amdgcn_readfirstlane(t >> 6), n = lane & 15, g = lane >> 4;
    unsigned par;
    if (!wait_par(rs, C_PAR, par)) return;
    const unsigned fb = rmap(L_FLAGS, (par & 1u) ^ 1u);
    unsigned got;
    if (!wait_word(rs, C_MODB, 40u, got)) return;
    unsigned seenU = 0, seenP = 0, seenW = 0;
    LAS unsigned* const TK = (LAS unsigned*)(lds + 65536);
    bar();
    if (t == 0) TK[0] = __hip_atomic_fetch_add((GAS unsigned*)(p.ws + C_OTK), 1u, __ATOMIC_RELAXED, __HIP_MEMORY_SCOPE_AGENT);
    bar();
    for (int it = 0;; ++it) {
        const int task = __builtin_amdgcn_readfirstlane((int)TK[it & 1]);
        if (task >= NSEG * 10) break;
        const int s = task / 10, gam = task % 10, c = gam * 4 + wave;
        unsigned nexttk = 0;
        if (t == 0 && s < OPRE) nexttk = __hip_atomic_fetch_add((GAS unsigned*)(p.ws + C_OTK), 1u, __ATOMIC_RELAXED, __HIP_MEMORY_SCOPE_AGENT);
        const v4u gau = ld16(rs, B_MOD + (2 * D + 16 * c + 4 * g) * 4);
        if (!wait_groups(rs, C_FU + wave * 128, 8, s + 1, lane, seenU)) return;
        peek_groups(rs, C_FW, 40, s + 1, lane, seenW);
        peek_groups(rs, C_FP + wave * 128, 8, s + 1, lane, seenP);
        v4u z[4][10], pf[2][10];
        auto load_p = [&]() {
#pragma unroll
            for (int i = 0; i < 10; ++i)
#pragma unroll
                for (int rt = 0; rt < 2; ++rt)
                    pf[rt][i] = i < 8 ? ld16s(rs, lane * 16, B_PX + (unsigned)((s * 2 + rt) * NH + wave * 8 + i) * 1024u)
                                      : ld16s(rs, lane * 16, B_PM + (unsigned)((s * 2 + rt) * 8 + wave * 2 + i - 8) * 1024u);
        };
        const bool pfirst = seenP >= (unsigned)(s + 1);
        if (pfirst) load_p();
#pragma unroll
        for (int i = 0; i < 8; ++i)
#pragma unroll
            for (int tl = 0; tl < 4; ++tl) z[tl][i] = ld16s(rs, lane * 16, B_ZX + (unsigned)((s * 40 + gam * 4 + tl) * NH + wave * 8 + i) * 1024u);
        v2u xr[2];
#pragma unroll
        for (int rt = 0; rt < 2; ++rt) xr[rt] = *(const v2u*)(p.x + (size_t)(32 * s + 16 * rt + n) * D + 16 * c + 4 * g);
        if (!wait_groups(rs, C_FW, 40, s + 1, lane, seenW)) return;
#pragma unroll
        for (int i = 0; i < 2; ++i)
#pragma unroll
            for (int tl = 0; tl < 4; ++tl) z[tl][8 + i] = ld16s(rs, lane * 16, B_ZM + (unsigned)(s * 40 + gam * 4 + tl) * 8192u + (wave * 2 + i) * 1024);
        if (!pfirst) {
            if (!wait_groups(rs, C_FP + wave * 128, 8, s + 1, lane, seenP)) return;
            load_p();
        }
        v4f acc[4][2];
#pragma unroll
        for (int i = 0; i < 10; ++i)
#pragma unroll
            for (int rt = 0; rt < 2; ++rt)
#pragma unroll
                for (int tl = 0; tl < 4; ++tl) acc[tl][rt] = mfma32(z[tl][i], pf[rt][i], i == 0 ? (v4f){0, 0, 0, 0} : acc[tl][rt]);
        char* RB = lds + (it & 1) * 32768;
        if (t == 0) TK[(it + 1) & 1] = s < OPRE ? nexttk : __hip_atomic_fetch_add((GAS unsigned*)(p.ws + C_OTK), 1u, __ATOMIC_RELAXED, __HIP_MEMORY_SCOPE_AGENT);
#pragma unroll
        for (int tl = 0; tl < 4; ++tl)
#pragma unroll
            for (int rt = 0; rt < 2; ++rt) *(LAS v4f*)(RB + ((wave * 8 + tl * 2 + rt) * 64 + lane) * 16) = acc[tl][rt];
        bar();
#pragma unroll
        for (int rt = 0; rt < 2; ++rt) {
            v4f a = {0, 0, 0, 0};
#pragma unroll
            for (int w = 0; w < 4; ++w) a += *(const LAS v4f*)(RB + ((w * 8 + wave * 2 + rt) * 64 + lane) * 16);
            const float r0 = bflo(xr[rt].x) + rb(__uint_as_float(gau.x) * rb(a[0])), r1 = bfhi(xr[rt].x) + rb(__uint_as_float(gau.y) * rb(a[1]));
            const float r2 = bflo(xr[rt].y) + rb(__uint_as_float(gau.z) * rb(a[2])), r3 = bfhi(xr[rt].y) + rb(__uint_as_float(gau.w) * rb(a[3]));
            st8s(rs, n * 32 + g * 8, B_X1 + (unsigned)(s * 40 + c) * 1024u + rt * 512, (v2u){pk2(r0, r1), pk2(r2, r3)});
        }
        drain();
        if (lane == 0) __hip_atomic_fetch_add((GAS unsigned*)(p.ws + C_OC + s * 4), 1u, __ATOMIC_RELAXED, __HIP_MEMORY_SCOPE_AGENT);
    }
}

DEV void role_Q(const Params& p, rsrc_t rs, int h, char* lds, int t) {
    const int lane = t & 63, wave = __builtin_amdgcn_readfirstlane(t >> 6), n = lane & 15, g = lane >> 4;
    v4u W[60];
    load_proj_weights<HD>(p.Wq + h * DH, lds, lane, wave, W);
    unsigned par;
    if (!wait_par(rs, C_PAR, par)) return;
    const unsigned fb = rmap(L_FLAGS, (par & 1u) ^ 1u);
    char* const SPB = lds + 81920;
    const int bfull = (wave & 1) ? (3 * wave + 1) / 2 : (3 * wave) / 2, bhalf = (wave & 1) ? (3 * wave - 1) / 2 : (3 * wave) / 2 + 1;
    const int gh = 2 * (wave & 1) + (g >> 1);
    v4u pf[10];
    unsigned seenK = 0, gotM = 0;
    bool owe = false;
    unsigned long long hmask = 0;
    if (!fetch_hf(rs, 0, t, hmask, pf)) return;
    put_hf(lds, t, pf);
    bar();
    for (int s = 0; s < NSEG; ++s) {
        const unsigned fv = ld4(rs, C_FKM + h * 128);
        if (s + 1 < NSEG) { if (!fetch_hf(rs, s + 1, t, hmask, pf)) return; }
        if (!wait_head(rs, C_FK, h, s + 1, lane, seenK)) return;
        const unsigned kb = B_KX + (unsigned)(s * NH + h) * 12288u;
        v4u kf[3]; v2u kh[3];
#pragma unroll
        for (int kt = 0; kt < 2; ++kt) {
            kf[kt] = ld16s(rs, lane * 16, kb + (kt * 6 + bfull) * 1024);
            kh[kt] = ld8s(rs, (gh * 16 + n) * 16 + (g & 1) * 8, kb + (kt * 6 + bhalf) * 1024);
        }
        const unsigned mb = B_KM + (unsigned)(s * NH + h) * 3072u;
        v4f acc[3][2];
        proj_mfma(lds + (s & 1) * 40960, lane, W, acc);
        gotM = __builtin_amdgcn_readfirstlane(fv);
        asm volatile("" ::: "memory");
        const bool early = (gotM >= (unsigned)(s + 1));
        if (early) { kf[2] = ld16s(rs, (g * 8 + (n & 7)) * 16, mb + bfull * 512); kh[2] = ld8s(rs, (gh * 8 + (n & 7)) * 16 + (g & 1) * 8, mb + bhalf * 512); }
        if (owe) { vmwait<2>(); if (lane < 2) st4(rs, C_FP + h * 16 + wave * 4 + lane * 8, s); }
        if (s + 1 < NSEG) put_hf(lds + ((s + 1) & 1) * 40960, t, pf);
        char* const SP = SPB + (s & 1) * 24576;
        v4u qf[2]; v2u qh[2];
#pragma unroll
        for (int rt = 0; rt < 2; ++rt) {
            qf[rt] = (v4u){pk2(acc[0][rt][0], acc[0][rt][1]), pk2(acc[0][rt][2], acc[0][rt][3]), pk2(acc[1][rt][0], acc[1][rt][1]), pk2(acc[1][rt][2], acc[1][rt][3])};
            qh[rt] = (v2u){pk2(acc[2][rt][0], acc[2][rt][1]), pk2(acc[2][rt][2], acc[2][rt][3])};
        }
        auto scores = [&](int kt) {
#pragma unroll
            for (int rt = 0; rt < 2; ++rt) {
                const v4f z4 = {0, 0, 0, 0};
                *(LAS v4f*)(SP + ((wave * 6 + rt * 3 + kt) * 64 + lane) * 16) = mfma32(kf[kt], qf[rt], z4) + mfma16(kh[kt], qh[rt], z4);
            }
        };
        scores(0); scores(1);
        if (!early) {
            if (!wait_word(rs, C_FKM + h * 128, s + 1, gotM)) return;
            kf[2] = ld16s(rs, (g * 8 + (n & 7)) * 16, mb + bfull * 512); kh[2] = ld8s(rs, (gh * 8 + (n & 7)) * 16 + (g & 1) * 8, mb + bhalf * 512);
        }
        scores(2);
        bar();
        if (wave < 2) {
            const int rt = wave;
            v4f sv[3];
#pragma unroll
            for (int kt = 0; kt < 3; ++kt) {
                sv[kt] = (v4f){0, 0, 0, 0};
#pragma unroll
                for (int w = 0; w < 4; ++w) sv[kt] += *(const LAS v4f*)(SP + ((w * 6 + rt * 3 + kt) * 64 + lane) * 16);
            }
#pragma unroll
            for (int kt = 0; kt < 3; ++kt)
#pragma unroll
                for (int i = 0; i < 4; ++i) sv[kt][i] = rb(rb(sv[kt][i]) * 0.07216878364870322f);
            const int tok = 16 * rt + n;
            const float NEG = -3.0e38f;
            float m = NEG;
#pragma unroll
            for (int i = 0; i < 4; ++i) {
                if (4 * g + i > tok) sv[0][i] = NEG;
                if (16 + 4 * g + i > tok) sv[1][i] = NEG;
                if (g >= 2) sv[2][i] = NEG;
                m = fmaxf(m, fmaxf(sv[0][i], fmaxf(sv[1][i], sv[2][i])));
            }
            m = xr16max(m); { float a = m, b = m; swap32(a, b); m = fmaxf(a, b); }
            float sum = 0;
#pragma unroll
            for (int kt = 0; kt < 3; ++kt)
#pragma unroll
                for (int i = 0; i < 4; ++i) { const float e = sv[kt][i] == NEG ? 0.f : __builtin_amdgcn_exp2f((sv[kt][i] - m) * 1.4426950408889634f); sv[kt][i] = e; sum += e; }
            sum = xr16sum(sum); { float a = sum, b = sum; swap32(a, b); sum = a + b; }
            const float inv = 1.0f / sum;
            const v4u o = {pk2(sv[0][0] * inv, sv[0][1] * inv), pk2(sv[0][2] * inv, sv[0][3] * inv), pk2(sv[1][0] * inv, sv[1][1] * inv), pk2(sv[1][2] * inv, sv[1][3] * inv)};
            st16s(rs, lane * 16, B_PX + (unsigned)((s * 2 + rt) * NH + h) * 1024u, o);
            unsigned a0 = pk2(sv[2][0] * inv, sv[2][1] * inv), a1 = pk2(sv[2][2] * inv, sv[2][3] * inv), b0 = a0, b1 = a1;
            swap16(a0, b0); swap16(a1, b1);
            if (lane < 16) st16s(rs, n * 16, B_PM + (unsigned)((s * 2 + rt) * NH + h) * 256u, (v4u){a0, a1, b0, b1});
        }
        owe = wave < 2 && early && s + 1 < NSEG;
        if (wave < 2 && !owe) { drain(); if (lane < 2) st4(rs, C_FP + h * 16 + wave * 4 + lane * 8, s + 1); }
    }
}

template <bool EXACT, class LD> DEV void ln_norm(LD ld, int lane, int wave, float (&f)[40]) {
    const int l16 = lane & 15, row = wave * 4 + (lane >> 4);
    v4u v[5];
#pragma unroll
    for (int i = 0; i < 5; ++i) v[i] = ld(row, l16 + 16 * i);
    float sum = 0;
#pragma unroll
    for (int i = 0; i < 5; ++i)
#pragma unroll
        for (int j = 0; j < 4; ++j) { f[i * 8 + 2 * j] = bflo(v[i][j]); f[i * 8 + 2 * j + 1] = bfhi(v[i][j]); sum += f[i * 8 + 2 * j] + f[i * 8 + 2 * j + 1]; }
    const float mean = sum16(sum) * (1.0f / D);
    float q = 0;
#pragma unroll
    for (int i = 0; i < 40; ++i) { const float d = f[i] - mean; q += d * d; }
    q = sum16(q);
    const float mb = rb(mean), sd = rb(sqrtf(rb(rb(q * (1.0f / D)) + 1e-5f)));
    const float inv = 1.0f / sd;
#pragma unroll
    for (int i = 0; i < 40; ++i) f[i] = EXACT ? rb(rb(f[i] - mb) / sd) : (f[i] - mb) * inv;
}
DEV void ln_coef(rsrc_t rs, unsigned shift_off, unsigned scale_off, int lane, float (&sh)[40], float (&sc)[40]) {
    const int l16 = lane & 15;
#pragma unroll
    for (int i = 0; i < 5; ++i)
#pragma unroll
        for (int q = 0; q < 2; ++q) {
            const v4u a = ld16(rs, shift_off + ((l16 + 16 * i) * 8 + q * 4) * 4), b = ld16(rs, scale_off + ((l16 + 16 * i) * 8 + q * 4) * 4);
#pragma unroll
            for (int j = 0; j < 4; ++j) { sh[i * 8 + q * 4 + j] = __uint_as_float(a[j]); sc[i * 8 + q * 4 + j] = rb(1.0f + __uint_as_float(b[j])); }
        }
}
template <bool EXACT> DEV void ln_apply(char* AF, int lane, int wave, const float (&f)[40], const float (&sh)[40], const float (&sc)[40]) {
    const int l16 = lane & 15, row = wave * 4 + (lane >> 4);
#pragma unroll
    for (int i = 0; i < 5; ++i) {
        v4u o;
#pragma unroll
        for (int j = 0; j < 4; ++j) {
            const int e = i * 8 + 2 * j;
            o[j] = EXACT ? pk2(rb(f[e] * sc[e]) + sh[e], rb(f[e + 1] * sc[e + 1]) + sh[e + 1]) : pk2(f[e] * sc[e] + sh[e], f[e + 1] * sc[e + 1] + sh[e + 1]);
        }
        const int ch = l16 + 16 * i;
        sts16(AF + ((ch >> 2) * 64 + (((ch & 3) * 16 + row) ^ (ch & 7))) * 16, o);
    }
}
template <int NC> DEV void mod_task(const Params& p, rsrc_t rs, int col0, unsigned counter, char* lds, int t) {
    constexpr int KG = 256 / NC, KP = D / KG;
    const int cc = t % NC, kg = t / NC;
    float acc[8] = {0, 0, 0, 0, 0, 0, 0, 0};
    v4u w[KP];
#pragma unroll
    for (int k = 0; k < KP; ++k) w[k] = *(const v4u*)(p.wmod + (size_t)(kg * KP + k) * (6 * D) + col0 + cc * 8);
#pragma unroll
    for (int k = 0; k < KP; ++k) {
        const float xc = __uint_as_float((unsigned)p.c[kg * KP + k] << 16);
        const float sl = rb(xc / (1.0f + __expf(-xc)));
#pragma unroll
        for (int j = 0; j < 4; ++j) { acc[2 * j] += sl * bflo(w[k][j]); acc[2 * j + 1] += sl * bfhi(w[k][j]); }
    }
    LAS float* red = (LAS float*)lds;
#pragma unroll
    for (int j = 0; j < 8; ++j) red[kg * (NC * 8 + 1) + cc * 8 + j] = acc[j];
    bar();
    if (t < 64) {
        if (t < NC * 8) {
            float a = 0;
#pragma unroll 8
            for (int k = 0; k < KG; ++k) a += red[k * (NC * 8 + 1) + t];
            st4(rs, B_MOD + (col0 + t) * 4, __float_as_uint(rb(a)));
        }
        drain();
        if (t == 0) __hip_atomic_fetch_add((GAS unsigned*)(p.ws + counter), 1u, __ATOMIC_RELAXED, __HIP_MEMORY_SCOPE_AGENT);
    }
    bar();
}
DEV bool prologue(const Params& p, rsrc_t rs, int pw, char* lds, int t) {
    const int lane = t & 63, wave = __builtin_amdgcn_readfirstlane(t >> 6);
    float sh[40], sc[40];
    for (int task = pw; task < 128; task += 48) {
        float f[40];
        const uint16_t* xs = p.x + (size_t)task * 16 * D;
        ln_norm<true>([&](int row, int ch) { return *(const v4u*)(xs + row * D + ch * 8); }, lane, wave, f);
        if (task == pw) {
            unsigned got;
            if (!wait_word(rs, C_MODA, 40u, got)) return false;
            ln_coef(rs, B_MOD, B_MOD + D * 4, lane, sh, sc);
        }
        ln_apply<true>(lds, lane, wave, f, sh, sc);
        bar();
#pragma unroll
        for (int k = 0; k < 5; ++k) st16s(rs, t * 16, B_HF + (unsigned)task * 20480u + k * 4096, lds16(lds + (k * 256 + t) * 16));
        drain();
        bar();
        if (t == 0) st4(rs, C_FH + (task & 1) * 256 + (task >> 1) * 4, 1u);
    }
    unsigned par;
    if (!wait_par(rs, C_PAR, par)) return false;
    const unsigned fb = rmap(L_FLAGS, (par & 1u) ^ 1u);
    if (pw < 40) mod_task<8>(p, rs, 2 * D + pw * 64, C_MODB, lds, t);
    return true;
}

DEV void helper(const Params& p, rsrc_t rs, int x, int i, char* lds, int t) {
    const int lane = t & 63, wave = __builtin_amdgcn_readfirstlane(t >> 6);
    if (i < 10) mod_task<4>(p, rs, (x * 10 + i) * 32, C_MODA, lds, t);
    const bool prober = x == 0 && i >= 10 && i < 14;
    if (prober && wave >= 2) {
        if (lane == 0) {
            const int cd = i - 10;
            unsigned best = 0xffffffffu;
            for (int r = 0; r < 10; ++r) {
                GAS unsigned* a = (GAS unsigned*)(p.ws + ((cd >> 1) ? PAGE + rmap(L_PROBE1, cd & 1) : rmap(L_PROBE0, cd & 1)) + ((r + wave) & 3) * 2048 + (wave & 1) * 128);
                unsigned z = 0, v; unsigned long long t0, t1;
                asm volatile("s_memtime %1\n\ts_waitcnt lgkmcnt(0)\n\tglobal_atomic_add %0, %3, %4, off sc0 sc1\n\ts_waitcnt vmcnt(0)\n\ts_memtime %2\n\ts_waitcnt lgkmcnt(0)"
                             : "=&v"(v), "=&s"(t0), "=&s"(t1) : "v"(a), "v"(z) : "memory");
                best = min(best, (unsigned)(t1 - t0));
            }
            st4(rs, C_PAR + (wave & 1) * 16 + cd * 4, best | 1u);
        }
        return;
    }
    const int nw = prober ? 2 : 4, h = 8 * x + (i & 7);
    v4u sink = {0, 0, 0, 0};
    if (i < 8) {
        for (int q = wave; q < 240; q += nw) {
            const int c = q * 64 + lane, ps = c / 1536, r = (c % 1536) / 24;
            const int ks = ps < 5 ? 2 * ps : 2 * ps - 9;
            const char* a = (const char*)(p.Wk + (size_t)(ks * 64 + r) * HD + h * DH) + (c % 24) * 16;
            asm volatile("global_load_dwordx4 %0, %1, off" : "+v"(sink) : "v"(a) : "memory");
        }
    } else {
        for (int q = wave; q < 240; q += nw) {
            const char* a = (const char*)(p.Wo + (size_t)h * DH * D) + (q * 64 + lane) * 16;
            asm volatile("global_load_dwordx4 %0, %1, off" : "+v"(sink) : "v"(a) : "memory");
        }
    }
    asm volatile("s_waitcnt vmcnt(0)" : "+v"(sink) :: "memory");
}

DEV void role_L(const Params& p, rsrc_t rs, int rt, char* lds, int t) {
    const int lane = t & 63, wave = __builtin_amdgcn_readfirstlane(t >> 6);
    unsigned par;
    if (!wait_par(rs, C_PAR, par)) return;
    const unsigned fb = rmap(L_FLAGS, (par & 1u) ^ 1u);
    unsigned got;
    if (!wait_word(rs, C_MODB, 40u, got)) return;
    float sh[40], sc[40];
    ln_coef(rs, B_MOD + 3 * D * 4, B_MOD + 4 * D * 4, lane, sh, sc);
    for (int s = 0; s < NSEG; ++s) {
        if (!wait_word(rs, C_OC + s * 4, 40u, got)) return;
        const unsigned xb = B_X1 + (unsigned)s * 40960u + rt * 512;
        float f[40];
        ln_norm<false>([&](int row, int ch) { return ld16s(rs, (ch >> 1) * 1024 + row * 32 + (ch & 1) * 16, xb); }, lane, wave, f);
        char* AF = lds + (s & 1) * 20480;
        ln_apply<false>(AF, lane, wave, f, sh, sc);
        bar();
#pragma unroll
        for (int k = 0; k < 5; ++k) st16s(rs, t * 16, B_H2 + (unsigned)(s * 2 + rt) * 20480u + k * 4096, lds16(AF + (k * 256 + t) * 16));
        post(rs, C_FL + rt * 16, wave, lane, s + 1);
    }
}

DEV void role_M1(const Params& p, rsrc_t rs, int phi, char* lds, int t) {
    const int lane = t & 63, wave = __builtin_amdgcn_readfirstlane(t >> 6), g = lane >> 4;
    v4u W[60];
    load_proj_weights<FF>(p.w1 + phi * 192, lds, lane, wave, W);
    unsigned par;
    if (!wait_par(rs, C_PAR, par)) return;
    const unsigned fb = rmap(L_FLAGS, (par & 1u) ^ 1u);
    const int efull = (wave & 1) ? 48 * wave + 16 : 48 * wave, ehalf = (wave & 1) ? 48 * wave : 48 * wave + 32;
    float bias[3][4];
#pragma unroll
    for (int i = 0; i < 4; ++i) {
        bias[0][i] = __uint_as_float((unsigned)p.b1[phi * 192 + efull + 8 * g + i] << 16);
        bias[1][i] = __uint_as_float((unsigned)p.b1[phi * 192 + efull + 8 * g + 4 + i] << 16);
        bias[2][i] = __uint_as_float((unsigned)p.b1[phi * 192 + ehalf + 4 * g + i] << 16);
    }
    unsigned seenL = 0;
    v4u pf[10];
    auto fetch = [&](int s) {
#pragma unroll
        for (int k = 0; k < 10; ++k) pf[k] = ld16s(rs, t * 16, B_H2 + (unsigned)s * 40960u + k * 4096);
    };
    if (!wait_groups(rs, C_FL, 2, 1, lane, seenL)) return;
    fetch(0); put_hf(lds, t, pf);
    bar();
    bool owe = false;
    for (int s = 0; s < NSEG; ++s) {
        const bool pre = s + 1 < NSEG && peek_groups(rs, C_FL, 2, s + 2, lane, seenL);
        if (pre) fetch(s + 1);
        v4f acc[3][2];
        proj_mfma(lds + (s & 1) * 40960, lane, W, acc);
        if (owe) post(rs, C_FM1 + phi * 16, wave, lane, s);
        if (pre) put_hf(lds + ((s + 1) & 1) * 40960, t, pf);
#pragma unroll
        for (int tl = 0; tl < 3; ++tl)
#pragma unroll
            for (int rt = 0; rt < 2; ++rt)
#pragma unroll
                for (int i = 0; i < 4; ++i) { float v = rb(rb(acc[tl][rt][i]) + bias[tl][i]); v = v > 0.f ? v : 0.f; acc[tl][rt][i] = v * v; }
        proj_store(rs, B_HID + (unsigned)(s * 8 + phi) * 12288u, lane, wave, acc);
        owe = pre;
        if (!pre) {
            post(rs, C_FM1 + phi * 16, wave, lane, s + 1);
            if (s + 1 < NSEG) {
                if (!wait_groups(rs, C_FL, 2, s + 2, lane, seenL)) return;
                fetch(s + 1); put_hf(lds + ((s + 1) & 1) * 40960, t, pf);
            }
        }
        bar();
    }
}

DEV void role_M2(const Params& p, rsrc_t rs, int gam, char* lds, int t) {
    const int lane = t & 63, wave = __builtin_amdgcn_readfirstlane(t >> 6), n = lane & 15, g = lane >> 4;
    v4u W[48];
    {
        const unsigned ldsb = (unsigned)(size_t)(LAS char*)lds + wave * 40960;
        unsigned v8[8];
#pragma unroll
        for (int k = 0; k < 8; ++k) { const int i = k * 64 + lane; v8[k] = (unsigned)((i >> 3) * D * 2 + stg_src<128>(i >> 3, i & 7) * 16); }
        auto issue8 = [&](int ps) {
            const uint16_t* src = p.w2 + (size_t)(wave * 384 + ps * 64) * D + gam * 64;
#pragma unroll
            for (int k = 0; k < 8; ++k) dma16(src, v8[k], ldsb + (ps % 5) * 8192 + k * 1024);
        };
#pragma unroll
        for (int ps = 0; ps < 5; ++ps) issue8(ps);
#pragma unroll
        for (int ps = 0; ps < 6; ++ps) {
            if (ps == 0) vmwait<32>(); else if (ps == 1) vmwait<32>(); else if (ps == 2) vmwait<24>(); else if (ps == 3) vmwait<16>(); else if (ps == 4) vmwait<8>(); else vmwait<0>();
            const char* tile = lds + wave * 40960 + (ps % 5) * 8192;
#pragma unroll
            for (int tl = 0; tl < 4; ++tl) {
                const v8u f = tr_frag<128>(tile, 0, lane, 16 * tl + 4 * (lane & 3));
                W[(2 * ps) * 4 + tl] = lo4(f); W[(2 * ps + 1) * 4 + tl] = hi4(f);
                asm volatile("" : "+a"(W[(2 * ps) * 4 + tl]), "+a"(W[(2 * ps + 1) * 4 + tl]));
            }
            if (ps == 0) { lgkm0(); issue8(5); }
        }
        bar();
    }
    const int col = gam * 64 + wave * 16 + 4 * g;
    const v2u b2u = *(const v2u*)(p.b2 + col);
    unsigned par;
    if (!wait_par(rs, C_PAR, par)) return;
    const unsigned fb = rmap(L_FLAGS, (par & 1u) ^ 1u);
    unsigned got;
    if (!wait_word(rs, C_MODB, 40u, got)) return;
    const v4u gbu = ld16(rs, B_MOD + (5 * D + col) * 4);
    unsigned seenM = 0;
    v4u nx[24];
    auto fetch = [&](int s) {
#pragma unroll
        for (int k = 0; k < 24; ++k) nx[k] = ld16s(rs, lane * 16, B_HID + (unsigned)(s * 8 + 2 * wave) * 12288u + k * 1024);
    };
    if (!wait_groups(rs, C_FM1 + wave * 32, 2, 1, lane, seenM)) return;
    fetch(0);
    for (int s = 0; s < NSEG; ++s) {
        v2u xr[2];
#pragma unroll
        for (int rt = 0; rt < 2; ++rt) xr[rt] = ld8s(rs, (16 * rt + n) * 32 + g * 8, B_X1 + (unsigned)(s * 40 + gam * 4 + wave) * 1024u);
        v4f acc[4][2];
#pragma unroll
        for (int kk = 0; kk < 12; ++kk)
#pragma unroll
            for (int rt = 0; rt < 2; ++rt) {
                const v4u b = nx[(kk / 6) * 12 + rt * 6 + kk % 6];
#pragma unroll
                for (int tl = 0; tl < 4; ++tl) { if (kk == 0) mf_av0(acc[tl][rt], W[tl], b); else mf_av(acc[tl][rt], W[kk * 4 + tl], b); }
            }
        const bool pre = s + 1 < NSEG && peek_groups(rs, C_FM1 + wave * 32, 2, s + 2, lane, seenM);
        if (pre) fetch(s + 1);
        settle2(acc[3][0], acc[3][1]); late3(acc[0][0], acc[1][0], acc[2][0]); late3(acc[0][1], acc[1][1], acc[2][1]);
        char* RB = lds + (s & 1) * 32768;
#pragma unroll
        for (int tl = 0; tl < 4; ++tl)
#pragma unroll
            for (int rt = 0; rt < 2; ++rt) *(LAS v4f*)(RB + ((wave * 8 + tl * 2 + rt) * 64 + lane) * 16) = acc[tl][rt];
        bar();
#pragma unroll
        for (int rt = 0; rt < 2; ++rt) {
            v4f a = {0, 0, 0, 0};
#pragma unroll
            for (int w = 0; w < 4; ++w) a += *(const LAS v4f*)(RB + ((w * 8 + wave * 2 + rt) * 64 + lane) * 16);
            const float r0 = bflo(xr[rt].x) + rb(__uint_as_float(gbu.x) * rb(rb(a[0]) + bflo(b2u.x))), r1 = bfhi(xr[rt].x) + rb(__uint_as_float(gbu.y) * rb(rb(a[1]) + bfhi(b2u.x)));
            const float r2 = bflo(xr[rt].y) + rb(__uint_as_float(gbu.z) * rb(rb(a[2]) + bflo(b2u.y))), r3 = bfhi(xr[rt].y) + rb(__uint_as_float(gbu.w) * rb(rb(a[3]) + bfhi(b2u.y)));
            *(v2u*)(p.out + (size_t)(32 * s + 16 * rt + n) * D + col) = (v2u){pk2(r0, r1), pk2(r2, r3)};
        }
        if (!pre && s + 1 < NSEG) {
            if (!wait_groups(rs, C_FM1 + wave * 32, 2, s + 2, lane, seenM)) return;
            fetch(s + 1);
        }
    }
}

__global__ void __launch_bounds__(256, 1) fused_kernel(Params p) {
    extern __shared__ __attribute__((aligned(16))) char lds[];
    const int t = threadIdx.x;
    const rsrc_t rs = __builtin_amdgcn_make_buffer_rsrc(p.ws, 0, B_END, 0x00020000);
    if (t < 64) {
        const unsigned x = (unsigned)__builtin_amdgcn_s_getreg(20 | (3 << 11)) & 7u;
        unsigned k = 0;
        if (t == 0) k = __hip_atomic_fetch_add((GAS unsigned*)(p.ws + C_TICK + x * 128), 1u, __ATOMIC_RELAXED, __HIP_MEMORY_SCOPE_AGENT);
        k = __builtin_amdgcn_readfirstlane(k);
        unsigned slot = x * 32 + k;
        if (k >= 32) {
            unsigned cnt[8];
            for (unsigned spins = 0;;) {
                const unsigned v = ld4(rs, C_TICK + (t & 7) * 128);
                asm volatile("" ::: "memory");
                unsigned tot = 0;
#pragma unroll
                for (int y = 0; y < 8; ++y) { cnt[y] = __builtin_amdgcn_readlane(v, y); tot += cnt[y]; }
                if (tot >= 256u || spin_fail(rs, spins)) break;
            }
            unsigned rank = k - 32;
#pragma unroll
            for (int y = 0; y < 8; ++y) if ((unsigned)y < x && cnt[y] > 32u) rank += cnt[y] - 32u;
            slot = 255;
#pragma unroll
            for (int y = 7; y >= 0; --y) {
                unsigned before = 0;
#pragma unroll
                for (int z = 0; z < 8; ++z) if (z < y && cnt[z] < 32u) before += 32u - cnt[z];
                if (cnt[y] < 32u && rank >= before && rank - before < 32u - cnt[y]) slot = y * 32 + cnt[y] + (rank - before);
            }
        }
        if (t == 0) *(LAS unsigned*)lds = slot;
    }
    bar();
    const int slot = __builtin_amdgcn_readfirstlane((int)*(LAS unsigned*)lds) & 255;
    const int sx = slot >> 5, sk = slot & 31;
    int role = sx < 4 ? (sk < 8 ? 8 * sx + sk : sk < 16 ? 32 + 8 * sx + (sk - 8) : sk < 26 ? 64 + 10 * sx + (sk - 16) : 104 + 6 * sx + (sk - 26))
                      : (sk < 12 ? 136 + 12 * (sx - 4) + sk : sk < 20 ? 184 + 8 * (sx - 4) + (sk - 12) : sk < 22 ? 128 + 2 * (sx - 4) + (sk - 20) : 216 + 4 * (sk - 22) + (sx - 4));
    bar();
    if (role >= 64 && role < 128) helper(p, rs, sx, sk - 16, lds, t);
    if (role >= 216 || (role >= 128 && role < 136)) {
        if (!prologue(p, rs, role >= 216 ? role - 216 : role - 88, lds, t)) return;
        bar();
    }
    if (role < 32) role_S(p, rs, role, lds, t);
    else if (role < 64) role_V(p, rs, role - 32, lds, t);
    else if (role < 104) role_R(p, rs, role - 64, lds, t);
    else if (role < 136) role_Q(p, rs, role - 104, lds, t);
    else if (role < 184) role_KX(p, rs, role - 136, lds, t);
    else if (role < 216) role_U(p, rs, role - 184, lds, t);
    else if (role < 236) {}
    else if (role < 244) role_M1(p, rs, role - 236, lds, t);
    else if (role < 254) role_M2(p, rs, role - 244, lds, t);
    else role_L(p, rs, role - 254, lds, t);
    if (role >= 136 && role < 236) role_O(p, rs, lds, t);
}

extern "C" void kernel_launch(void* const* d_in, const int* in_sizes, int n_in, void* d_out, int out_size, void* d_ws, size_t ws_size, hipStream_t stream) {
    Params p;
    p.x = (const uint16_t*)d_in[0]; p.mem0 = (const uint16_t*)d_in[1];
    p.Wq = (const uint16_t*)d_in[2]; p.Wk = (const uint16_t*)d_in[3]; p.Wv = (const uint16_t*)d_in[4]; p.Wo = (const uint16_t*)d_in[5];
    p.w1 = (const uint16_t*)d_in[8]; p.b1 = (const uint16_t*)d_in[9]; p.w2 = (const uint16_t*)d_in[10]; p.b2 = (const uint16_t*)d_in[11];
    p.c = (const uint16_t*)d_in[12]; p.wmod = (const uint16_t*)d_in[13];
    p.out = (uint16_t*)d_out;
    p.ws = (unsigned char*)(((uintptr_t)d_ws + PAGE - 1) & ~(uintptr_t)(PAGE - 1));
    (void)hipFuncSetAttribute((const void*)fused_kernel, hipFuncAttributeMaxDynamicSharedMemorySize, LDS_BYTES);
        (void)hipMemsetAsync(p.ws, 0, ZERO_BYTES, stream);
    hipLaunchKernelGGL(fused_kernel, dim3(256), dim3(256), LDS_BYTES, stream, p);
}
```
